```python
import torch

def _softmax(x, axis=-1):
    return torch.softmax(x, dim=axis)
def _gelu(x):
    return torch.nn.functional.gelu(x, approximate="tanh")
def _silu(x):
    return torch.nn.functional.silu(x)
def _sigmoid(x):
    return torch.sigmoid(x)
def _layernorm(x, axis=-1, eps=1e-5):
    m = x.mean(dim=axis, keepdim=True)
    v = x.var(dim=axis, keepdim=True, unbiased=False)
    return (x - m) / torch.sqrt(v + eps)
def _scatter_add(tgt, idx, src, axis=0):
    return tgt.clone().index_add_(axis, idx, src)
def _segment_sum(src, seg, n):
    out = torch.zeros((n,) + src.shape[1:], dtype=src.dtype)
    return out.index_add_(0, seg, src)
def _pad(x, pw):
    flat = tuple(v for pair in reversed(pw) for v in pair)
    return torch.nn.functional.pad(x, flat)

kernel_name = 'interleave_layers__gqa_block_cached__sliding_block_cached__decode'

def setup_inputs():

    _w = (lambda a: (lambda t: t.to(torch.bfloat16) if t.is_floating_point() else t)(torch.as_tensor(a)))
    def _A_setup():
        B, Sq, D = 1, 1, 448
        Hq, Hkv, Sk, Dh = 16, 8, 16384, 256
        assert Hq % Hkv == 0, (Hq, Hkv)
        return {
            "x": _w((lambda *s: torch.randn(*s, dtype=torch.bfloat16) * 0.1)(B, Sq, D)),
            "k_cache": _w((lambda *s: torch.randn(*s, dtype=torch.bfloat16) * 0.8)(B, Hkv, Sk, Dh)),
            "v_cache": _w((lambda *s: torch.randn(*s, dtype=torch.bfloat16) * 3.2)(B, Hkv, Sk, Dh)),
            "wq": _w((lambda *s: torch.randn(*s, dtype=torch.bfloat16) * 0.8)(D, Hq * Dh) * (D ** -0.5)),
            "wo": _w((lambda *s: torch.randn(*s, dtype=torch.bfloat16) * 3.2)(Hq * Dh, D) * ((Hq * Dh) ** -0.5)),
        }
    _A_in = _A_setup()

    def _B_setup():
        B, Sq, D = 1, 1, 448
        Hq, Hkv, Sk, Dh = 16, 8, 16384, 256
        assert Hq % Hkv == 0, (Hq, Hkv)
        return {
            "x": _w((lambda *s: torch.randn(*s, dtype=torch.bfloat16) * 0.1)(B, Sq, D)),
            "k_cache": _w((lambda *s: torch.randn(*s, dtype=torch.bfloat16) * 1.6)(B, Hkv, Sk, Dh)),
            "v_cache": _w((lambda *s: torch.randn(*s, dtype=torch.bfloat16) * 1.6)(B, Hkv, Sk, Dh)),
            "wq": _w((lambda *s: torch.randn(*s, dtype=torch.bfloat16) * 1.6)(D, Hq * Dh) * (D ** -0.5)),
            "wo": _w((lambda *s: torch.randn(*s, dtype=torch.bfloat16) * 1.6)(Hq * Dh, D) * ((Hq * Dh) ** -0.5)),
        }
    _B_in = _B_setup()

    return {
        "A_x": _A_in["x"],
        "A_k_cache": _A_in["k_cache"],
        "A_v_cache": _A_in["v_cache"],
        "A_wq": _A_in["wq"],
        "A_wo": _A_in["wo"],
        "B_x": _B_in["x"],
        "B_k_cache": _B_in["k_cache"],
        "B_v_cache": _B_in["v_cache"],
        "B_wq": _B_in["wq"],
        "B_wo": _B_in["wo"],
    }

def reference(A_x, A_k_cache, A_v_cache, A_wq, A_wo, B_x, B_k_cache, B_v_cache, B_wq, B_wo):
    def _A_ref(x, k_cache, v_cache, wq, wo):
        B, Sq, D = x.shape
        Hq, Dh = 16, 256
        Hkv = k_cache.shape[1]
        rep = Hq // Hkv
        q = (lambda x, *d: x.permute(*d))((x @ wq).reshape(B, Sq, Hq, Dh), 0, 2, 1, 3)
        k = (lambda x, n, axis: torch.repeat_interleave(x, n, dim=axis))(k_cache, rep, axis=1)
        v = (lambda x, n, axis: torch.repeat_interleave(x, n, dim=axis))(v_cache, rep, axis=1)
        scores = torch.einsum("bhqd,bhkd->bhqk", q, k) * (Dh ** -0.5)
        p = _softmax(scores, axis=-1)
        o = torch.einsum("bhqk,bhkd->bhqd", p, v)
        return (lambda x, *d: x.permute(*d))(o, 0, 2, 1, 3).reshape(B, Sq, Hq * Dh) @ wo

    def _B_ref(x, k_cache, v_cache, wq, wo):
        B, Sq, D = x.shape
        Hq, Dh = 16, 256
        Hkv, Sk = k_cache.shape[1], k_cache.shape[2]
        W = min(4096, max(256, Sk // 4))
        rep = Hq // Hkv
        q = (lambda x, *d: x.permute(*d))((x @ wq).reshape(B, Sq, Hq, Dh), 0, 2, 1, 3)
        k = (lambda x, n, axis: torch.repeat_interleave(x, n, dim=axis))(k_cache[:, :, Sk - W:, :], rep, axis=1)
        v = (lambda x, n, axis: torch.repeat_interleave(x, n, dim=axis))(v_cache[:, :, Sk - W:, :], rep, axis=1)
        scores = torch.einsum("bhqd,bhkd->bhqk", q, k) * (Dh ** -0.5)
        p = _softmax(scores, axis=-1)
        o = torch.einsum("bhqk,bhkd->bhqd", p, v)
        return (lambda x, *d: x.permute(*d))(o, 0, 2, 1, 3).reshape(B, Sq, Hq * Dh) @ wo

    x = A_x
    x = x + _A_ref(_layernorm(x, axis=-1), A_k_cache, A_v_cache, A_wq, A_wo)
    x = x + _B_ref(_layernorm(x, axis=-1), B_k_cache, B_v_cache, B_wq, B_wo)
    x = x + _A_ref(_layernorm(x, axis=-1), A_k_cache, A_v_cache, A_wq, A_wo)
    x = x + _B_ref(_layernorm(x, axis=-1), B_k_cache, B_v_cache, B_wq, B_wo)
    return x
```

```cpp
#include <hip/hip_runtime.h>
#include <hip/hip_bf16.h>
#include <stdint.h>

typedef unsigned short u16;
typedef unsigned v4u __attribute__((ext_vector_type(4)));
typedef float f32x4 __attribute__((ext_vector_type(4)));
typedef float f32x2 __attribute__((ext_vector_type(2)));
typedef __bf16 bf16x8 __attribute__((ext_vector_type(8)));
typedef __bf16 bf16x2 __attribute__((ext_vector_type(2)));

constexpr int DM = 448, HQ = 16, HKV = 8, SK = 16384, DH = 256, WIN = 4096, NQ = HQ * DH;

__device__ __forceinline__ float bf2f(u16 b) { return __builtin_bit_cast(float, (unsigned)b << 16); }
__device__ __forceinline__ unsigned cvtpk(float lo, float hi) {
  f32x2 v = {lo, hi}; bf16x2 b = __builtin_convertvector(v, bf16x2); return __builtin_bit_cast(unsigned, b);
}
__device__ __forceinline__ float rbf(float x) { return __builtin_bit_cast(float, cvtpk(x, x) << 16); }
__device__ __forceinline__ f32x4 mfma(v4u a, v4u b, f32x4 c) {
  return __builtin_amdgcn_mfma_f32_16x16x32_bf16(__builtin_bit_cast(bf16x8, a), __builtin_bit_cast(bf16x8, b), c, 0, 0, 0);
}

__global__ void __launch_bounds__(256) k_q(const u16* __restrict__ x, const u16* __restrict__ wq, u16* __restrict__ q) {
  __shared__ float xs[DM]; __shared__ float red[4][64]; __shared__ float st[2];
  int tid = threadIdx.x, lane = tid & 63, w = tid >> 6;
  for (int i = tid; i < DM; i += 256) xs[i] = bf2f(x[i]);
  __syncthreads();
  if (w == 0) {
    float s = 0.f;
    for (int i = lane; i < DM; i += 64) s += xs[i];
    for (int o = 32; o; o >>= 1) s += __shfl_xor(s, o);
    float m = s / DM; float v = 0.f;
    for (int i = lane; i < DM; i += 64) { float d = xs[i] - m; v += d * d; }
    for (int o = 32; o; o >>= 1) v += __shfl_xor(v, o);
    v /= DM;
    if (lane == 0) { st[0] = rbf(m); st[1] = rbf(sqrtf(rbf(rbf(v) + 1e-5f))); }
  }
  __syncthreads();
  float m = st[0], sd = st[1];
  for (int i = tid; i < DM; i += 256) xs[i] = rbf(rbf(xs[i] - m) / sd);
  __syncthreads();
  int col = blockIdx.x * 64 + lane; float acc = 0.f;
  for (int r = w * 112; r < w * 112 + 112; ++r) acc += xs[r] * bf2f(wq[(size_t)r * NQ + col]);
  red[w][lane] = acc; __syncthreads();
  if (w == 0) { float t = red[0][lane] + red[1][lane] + red[2][lane] + red[3][lane]; q[col] = (u16)(cvtpk(t, t) & 0xffff); }
}

template <int NB>
__global__ void __launch_bounds__(256) k_attn(const u16* __restrict__ q, const u16* __restrict__ K, const u16* __restrict__ V, int key_off,
                                              float* __restrict__ po, float* __restrict__ pml) {
  int tid = threadIdx.x, lane = tid & 63, w = tid >> 6;
  int kvh = blockIdx.x & 7, split = blockIdx.x >> 3;
  int i = lane & 15, g = lane >> 4;
  size_t key0 = (size_t)kvh * SK + key_off + (size_t)(split * 4 + w) * NB * 16;
  const char* kb = (const char*)K + key0 * 512 + i * 512 + g * 16;
  const char* vb = (const char*)V + key0 * 512 + (size_t)g * 4 * 512 + i * 16;
  v4u qf[8];
#pragma unroll
  for (int s = 0; s < 8; ++s) qf[s] = *(const v4u*)((const char*)q + (2 * kvh + (i >> 3)) * 512 + s * 64 + g * 16);
  f32x4 S[NB];
#pragma unroll
  for (int b = 0; b < NB; ++b) {
    f32x4 a = {0.f, 0.f, 0.f, 0.f};
#pragma unroll
    for (int s = 0; s < 8; ++s) { v4u kf = *((const v4u*)(kb + b * 8192 + s * 64)); a = mfma(kf, qf[s], a); }
    S[b] = a;
  }
  const float C = 0.0625f * 1.4426950408889634f;
  float m = -INFINITY;
#pragma unroll
  for (int b = 0; b < NB; ++b)
#pragma unroll
    for (int r = 0; r < 4; ++r) m = fmaxf(m, S[b][r]);
  m = fmaxf(m, __shfl_xor(m, 16)); m = fmaxf(m, __shfl_xor(m, 32));
  m *= C;
  float l = 0.f;
  unsigned msk[4];
#pragma unroll
  for (int k = 0; k < 4; ++k) msk[k] = ((i & 7) >> 1) == k ? ((i & 1) ? 0xffff0000u : 0x0000ffffu) : 0u;
  f32x4 acc0 = {0.f, 0.f, 0.f, 0.f}, acc1 = {0.f, 0.f, 0.f, 0.f};
#pragma unroll
  for (int b = 0; b < NB; ++b)
#pragma unroll
    for (int r = 0; r < 4; ++r) {
      float p = __builtin_amdgcn_exp2f(S[b][r] * C - m);
      l += p;
      unsigned pk = cvtpk(p, p);
      v4u a = {pk & msk[0], pk & msk[1], pk & msk[2], pk & msk[3]};
      v4u v0 = *((const v4u*)(vb + b * 8192 + r * 512));
      v4u v1 = *((const v4u*)(vb + b * 8192 + r * 512 + 256));
      acc0 = mfma(a, v0, acc0); acc1 = mfma(a, v1, acc1);
    }
  l += __shfl_xor(l, 16); l += __shfl_xor(l, 32);
  size_t slot = (size_t)(kvh * 32 + split) * 4 + w;
  float* o = po + slot * 512 + (g >> 1) * 256 + 8 * i + 4 * (g & 1);
  *(f32x4*)o = acc0; *(f32x4*)(o + 128) = acc1;
  if (lane == 0) { pml[slot * 4 + 0] = m; pml[slot * 4 + 1] = l; }
  if (lane == 8) { pml[slot * 4 + 2] = m; pml[slot * 4 + 3] = l; }
}

__global__ void __launch_bounds__(256) k_comb(const float* __restrict__ po, const float* __restrict__ pml, float* __restrict__ o) {
  int h = blockIdx.x, kvh = h >> 1, hh = h & 1, d = threadIdx.x;
  float M = -INFINITY;
  for (int s = 0; s < 128; ++s) M = fmaxf(M, pml[(size_t)(kvh * 128 + s) * 4 + hh * 2]);
  float L = 0.f, a = 0.f;
  for (int s = 0; s < 128; ++s) {
    float wgt = __builtin_amdgcn_exp2f(pml[(size_t)(kvh * 128 + s) * 4 + hh * 2] - M);
    L += wgt * pml[(size_t)(kvh * 128 + s) * 4 + hh * 2 + 1];
    a += wgt * po[(size_t)(kvh * 128 + s) * 512 + hh * 256 + d];
  }
  o[h * 256 + d] = rbf(a / L);
}
__global__ void __launch_bounds__(1024) k_wo(const float* __restrict__ o, const u16* __restrict__ wo, const u16* __restrict__ x, u16* __restrict__ xo) {
  __shared__ float red[16][64];
  int lane = threadIdx.x & 63, w = threadIdx.x >> 6, col = blockIdx.x * 64 + lane;
  float acc = 0.f;
  for (int r = w * 256; r < w * 256 + 256; ++r) acc += o[r] * bf2f(wo[(size_t)r * DM + col]);
  red[w][lane] = acc; __syncthreads();
  if (w == 0) { float t = 0.f; for (int k = 0; k < 16; ++k) t += red[k][lane]; float y = rbf(t); float z = bf2f(x[col]) + y; xo[col] = (u16)(cvtpk(z, z) & 0xffff); }
}

extern "C" void kernel_launch(void* const* d_in, const int* in_sizes, int n_in, void* d_out, int out_size, void* d_ws, size_t ws_size, hipStream_t stream) {
  const u16* x0 = (const u16*)d_in[0];
  const u16* Kc[2] = {(const u16*)d_in[1], (const u16*)d_in[6]};
  const u16* Vc[2] = {(const u16*)d_in[2], (const u16*)d_in[7]};
  const u16* wq[2] = {(const u16*)d_in[3], (const u16*)d_in[8]};
  const u16* wo[2] = {(const u16*)d_in[4], (const u16*)d_in[9]};
  char* ws = (char*)d_ws;
  u16* q = (u16*)ws; float* o = (float*)(ws + 65536); u16* xb = (u16*)(ws + 131072);
  float* pml = (float*)(ws + 262144); float* po = (float*)(ws + 1048576);
  const u16* xin = x0;
  for (int L = 0; L < 4; ++L) {
    int t = L & 1;
    u16* xout = (L == 3) ? (u16*)d_out : xb + L * 512;
    k_q<<<64, 256, 0, stream>>>(xin, wq[t], q);
    if (t == 0) k_attn<8><<<256, 256, 0, stream>>>(q, Kc[0], Vc[0], 0, po, pml);
    else k_attn<2><<<256, 256, 0, stream>>>(q, Kc[1], Vc[1], SK - WIN, po, pml);
    k_comb<<<16, 256, 0, stream>>>(po, pml, o);
    k_wo<<<7, 1024, 0, stream>>>(o, wo[t], xin, xout);
    xin = xout;
  }
}
```

```cpp
#define CODE_BYTES 10240
#define ROT ((w+sp)&3)
#define PREY 5
#define PREQ 5
#define PRES 3
#define PREN 0
#define PF 0
#include <hip/hip_runtime.h>
#include <hip/hip_bf16.h>
#include <stdint.h>

namespace fk {
typedef unsigned v4u __attribute__((ext_vector_type(4)));
typedef __amdgpu_buffer_rsrc_t srd_t;
#define DI __device__ __forceinline__
#define LAS __attribute__((address_space(3)))

constexpr unsigned KV_BYTES = 8u * 16384u * 512u, W_BYTES = 448u * 4096u * 2u;
constexpr unsigned LREG = 0x100000, O_PO = 0, O_ML = 0x40000, O_YQ = 2 * LREG + 0xA0000, O_YB = 0, O_Q = 0x4000;
constexpr unsigned O_CTRL = 0x400000, O_RDY = 0x401000, O_STAMP = 0x410000, WS_USED = 0x450000;
constexpr unsigned L_MRG = 131072, L_TAB = L_MRG + 8192, L_R1 = L_TAB + 16384, L_R2 = L_R1 + 3072 + 512, L_MLX = L_R2 + 1024, L_ST = L_MLX + 64;
static_assert(L_ST == 160320 && L_ST + 32 <= 163840, "lds map");
constexpr unsigned LDS_BYTES = 163840;
constexpr unsigned GOOB = 0x7FFF0000u;
constexpr unsigned L_DUM = 160512;

DI unsigned amap(unsigned o) { return (o & 0x1FFFu) | ((o >> 13) << 14); }
template <int REG, int IMM> DI void ldK(unsigned voff, srd_t srd, unsigned soff) {
  asm volatile("buffer_load_dwordx4 a[%c3:%c4], %0, %1, %2 offen offset:%c5 nt" ::"v"(voff), "s"(srd), "s"(soff), "i"(REG), "i"(REG + 3), "i"(IMM) : "memory");
}
template <int B, int O> DI void octK(unsigned vk, srd_t r, unsigned s) {
  __builtin_amdgcn_sched_barrier(0);
  ldK<32 * B + 16 * O, 0>(vk, r, s + B * 32768 + O * 4096); ldK<32 * B + 16 * O + 4, 128>(vk, r, s + B * 32768 + O * 4096);
  ldK<32 * B + 16 * O + 8, 256>(vk, r, s + B * 32768 + O * 4096); ldK<32 * B + 16 * O + 12, 384>(vk, r, s + B * 32768 + O * 4096);
  __builtin_amdgcn_sched_barrier(0);
}
template <int B> DI void blkK(unsigned vk, srd_t r, unsigned s) {
  ldK<32 * B, 0>(vk, r, s + B * 32768); ldK<32 * B + 4, 128>(vk, r, s + B * 32768); ldK<32 * B + 8, 256>(vk, r, s + B * 32768); ldK<32 * B + 12, 384>(vk, r, s + B * 32768);
  ldK<32 * B + 16, 0>(vk, r, s + B * 32768 + 4096); ldK<32 * B + 20, 128>(vk, r, s + B * 32768 + 4096);
  ldK<32 * B + 24, 256>(vk, r, s + B * 32768 + 4096); ldK<32 * B + 28, 384>(vk, r, s + B * 32768 + 4096);
}
DI srd_t mk(const void* p, unsigned bytes) { return __builtin_amdgcn_make_buffer_rsrc((void*)p, 0, bytes, 0x00020000); }

__global__ void __launch_bounds__(256, 1) __attribute__((amdgpu_num_vgpr(64)))
fused(const char* __restrict__ xin, const char* __restrict__ KA, const char* __restrict__ VA, const char* __restrict__ wqA, const char* __restrict__ woA,
      const char* __restrict__ KB, const char* __restrict__ VB, const char* __restrict__ wqB, const char* __restrict__ woB, char* __restrict__ out, char* wsraw) {
  extern __shared__ __attribute__((aligned(16))) char smem[];
  LAS char* L = (LAS char*)smem;
  asm volatile("" ::: "v255", "a255");
  unsigned t00 = 0;
  const unsigned c = blockIdx.x, kvh = c & 7, sp = c >> 3;
  const unsigned w = __builtin_amdgcn_readfirstlane(threadIdx.x >> 6), l = threadIdx.x & 63;
  char* ws = (char*)(((uintptr_t)wsraw + 0x1FFFFFu) & ~(uintptr_t)0x1FFFFFu);
  const srd_t rWS = mk(ws, WS_USED);
  unsigned vote = 0;
  if (__builtin_expect(!(kvh & 1) && sp >= 28, 0)) {
    if (w == 0) {
      const unsigned base = LREG + 0xA0000 + ((sp - 28) * 4 + (kvh >> 1)) * 0x4000, vo = l < 8 ? l * 16 : GOOB;
      const v4u z = {0u, 0u, 0u, 0u};
      __builtin_amdgcn_raw_buffer_store_b128(z, rWS, vo, base + 0x1F80, 16);
      __builtin_amdgcn_raw_buffer_store_b128(z, rWS, vo, base + 0x1F80 + 2 * LREG, 16);
      unsigned best[4] = {~0u, ~0u, ~0u, ~0u};
#pragma unroll 1
      for (unsigned rep = 0; rep < 1; ++rep) {
#pragma unroll
        for (int k = 0; k < 4; ++k) {
          unsigned long long a, b;
          asm volatile("s_waitcnt vmcnt(0)\n\ts_memtime %0\n\ts_waitcnt lgkmcnt(0)" : "=s"(a)::"memory");
          __builtin_amdgcn_raw_buffer_store_b128(z, rWS, vo, base + (k >> 1) * (2 * LREG) + (k & 1) * 0x2000 + rep * 0x1080, 16);
          asm volatile("s_waitcnt vmcnt(0)\n\ts_memtime %0\n\ts_waitcnt lgkmcnt(0)" : "=s"(b)::"memory");
          best[k] = min(best[k], (unsigned)(b - a));
        }
      }
      const unsigned half = __builtin_amdgcn_s_getreg(20 | (2 << 6) | (0 << 11));
      vote = (((best[1] < best[0] ? 1u : 0u) ^ half) << 31) | (((best[3] < best[2] ? 1u : 0u) ^ half) << 30);
    }
    __builtin_amdgcn_s_barrier();
  }
  const srd_t rKA = mk(KA, KV_BYTES);
  const unsigned sa = (kvh * 16384 + sp * 512 + w * 16) * 512u;
  const unsigned rot = ROT;
  const unsigned vK = ((l + 2 * rot) & 7) * 512 + ((l >> 3) & 1) * 64 + (l >> 4) * 16;
  blkK<0>(vK, rKA, sa);
  __builtin_amdgcn_sched_barrier(0);
  const srd_t rX = mk(xin, 896);
  const unsigned vx = (l & 8) ? GOOB : w * 256 + (l >> 5) * 128 + (l & 7) * 16 + ((l >> 4) & 1) * 8;
  asm volatile("buffer_load_dwordx2 v[254:255], %0, %1, 0 offen" ::"v"(vx), "s"(rX) : "memory");
  {
    unsigned long long pc;
    asm volatile("s_getpc_b64 %0" : "=s"(pc));
    const srd_t rC = mk((const void*)(pc & ~63ull), CODE_BYTES);
    asm volatile("buffer_load_dwordx4 v[248:251], %0, %1, 0 offen" ::"v"(((sp * 4 + w) & 15) * 1024 + l * 16), "s"(rC) : "memory");
  }
  __builtin_amdgcn_sched_barrier(0);

  const unsigned pair = (kvh >> 1) ^ 1;
  const bool is_q = (kvh & 1) != 0, is_y = !is_q && sp < 28;
  const unsigned role = is_q ? 1 : is_y ? 0 : 2;
  const unsigned s7 = sp < 28 ? sp : 27, head = pair * 4 + s7 / 7, cb = s7 % 7, jq = pair * 32 + sp;
  const unsigned sb = (kvh * 16384 + 12288 + sp * 128 + w * 16) * 512u;
  const unsigned n = l & 15, g = l >> 4, j = l & 7;
  const unsigned LOOB = L_DUM + l * 16;
  const unsigned vV = (g >> 1) * 4096 + (g & 1) * 512 + n * 16;
  const unsigned vLA = w * 32768 + l * 16;
  const unsigned hm = (l & 8) ? 0xFFFF0000u : 0x0000FFFFu;
  const unsigned vM0 = (j >> 2) == 0 ? hm : 0u, vM1 = (j >> 2) == 1 ? hm : 0u, vIDX = (j & 3) * 0x5555u;
  const unsigned vMW = L_MRG + w * 2048 + l * 8, vMR = L_MRG + w * 512 + l * 8;
  const unsigned rec = (kvh * 2 + (l >> 5)) * 32 + sp;
  const unsigned vPOst = amap(O_PO + rec * 512 + w * 128 + (l & 31) * 4);
  const unsigned vMLst = (w == 0 && (l & 30) == 0) ? amap(O_ML + rec * 128) + (l & 1) * 0x2000 : GOOB;
  const unsigned vMLX = L_MLX + (l >> 5) * 32 + (l & 3) * 8, vMLXw = (l & 31) == 0 ? L_MLX + (l >> 5) * 32 + w * 8 : LOOB;
  const unsigned vQg = O_YQ + amap(O_Q + kvh * 2048) + (l >> 2) * 128 + (l & 3) * 16;
  const unsigned vQw = L_MRG + w * 2048 + l * 16 + (l >> 5) * 128, vQr = L_MRG + w * 2048 + (l & 1) * 640 + ((l >> 1) & 1) * 64 + g * 16;
  const unsigned u = n >> 3;
  const unsigned vW = is_q ? (32 * (g >> 1) + 4 * (g & 1) + u) * 8192 + j * 8 : (64 * (g >> 1) + 8 * (g & 1) + u) * 896 + j * 16;
  const unsigned wb = is_q ? w * 128u * 8192u + jq * 64u : (head * 256 + 128 * (w >> 1) + 2 * (w & 1)) * 896u + cb * 128u;
  const unsigned vTAB = L_TAB + w * 4096 + l * 16;
  octK<1, 0>(vK, rKA, sa);
  const unsigned rsp = 2 * ((3u - (unsigned)((uintptr_t)wqA >> 7)) & 7u), ye = (kvh >> 1) * 28 + sp, ni = (sp - 28) * 4 + (kvh >> 1);
  const unsigned jx = is_q ? jq : is_y ? 16 * (ye / 14) + ((rsp + 2 + ye % 14) & 15) : 16 * (ni >> 1) + rsp + (ni & 1);
  const unsigned vWq = (32 * (g >> 1) + 4 * (g & 1) + u) * 8192 + j * 8, wbq = w * 128u * 8192u + jx * 64u;
  v4u t0, t1, t2;
  t0.x = O_YQ + amap(O_YB + w * 4096) + l * 16;
  t0.y = w == 3 ? GOOB : t0.x + 2048;
  t0.z = (l & 0x37) == 0 ? L_ST + w * 8 + (l >> 3) * 4 : LOOB;
  t0.w = u == 0 ? L_R1 + w * 256 + (g >> 1) * 128 + j * 16 + (g & 1) * 8 : LOOB;
  t1.x = L_R1 + w * 256 + g * 64;
  t1.y = u == 0 ? L_R2 + w * 256 + (2 * j + (g & 1)) * 16 + (g >> 1) * 8 : LOOB;
  t1.z = L_R2 + n * 16;
  t1.w = g < 2 ? O_YQ + amap(O_Q + jx * 128) + n * 4 + g * 0x2000 : GOOB;
  const unsigned vPRO = L_MRG + w * 2048 + l * 16;
  if (!is_q) {
    *(LAS v4u*)(L + vPRO) = t0;
    *(LAS v4u*)(L + vPRO + 1024) = t1;
    const unsigned m = 4 * j + 2 * (g & 1) + (g >> 1);
    t0.x = amap(O_PO + head * 16384) + (l >> 3) * 512 + w * 128 + (l & 7) * 16;
    t0.y = t0.x + 4096;
    t0.z = amap(O_ML + head * 4096) + (4 * g + (n >> 3)) * 128;
    t0.w = u == 0 ? L_R1 + w * 256 + m * 4 : LOOB;
    t1.x = L_R1 + w * 256 + g * 32;
    t1.y = u == 0 ? L_R2 + w * 256 + m * 8 : LOOB;
    t1.z = L_R2 + (l & 31) * 8;
    t1.w = O_YQ + amap(O_YB + (cb * 16 + head) * 128) + (l & 31) * 4 + (l >> 5) * 0x2000;
  }
  {
    const unsigned cpy = ((l >> 3) & 1) * 0x2000, lay = l >> 4;
    t2.x = amap(O_PO + rec * 512 + (l & 31) * 16);
    t2.y = t2.x + 0x2000;
    t2.z = j < 2 ? amap(O_ML + (kvh * 64 + j * 32 + sp) * 128) + cpy + lay * LREG : GOOB;
    t2.w = is_y ? O_YQ + amap(O_YB + (cb * 16 + head) * 128) + j * 16 + cpy + lay * 0x10000
                : (is_q && lay != 0) ? O_YQ + amap(O_Q + jq * 128) + j * 16 + cpy + lay * 0x10000 : GOOB;
  }
  *(LAS v4u*)(L + vTAB) = t0;
  *(LAS v4u*)(L + vTAB + 1024) = t1;
  *(LAS v4u*)(L + vTAB + 2048) = t2;
  *(LAS v4u*)(L + vTAB + 3072) = v4u{vx, u ? 0u : (n & 4) ? 0xFFFF0000u : 0x0000FFFFu, u ? 0x3F80u : 0u, 0u};
  octK<1, 1>(vK, rKA, sa);
  const srd_t rVA = mk(VA, KV_BYTES), rKB = mk(KB, KV_BYTES), rVB = mk(VB, KV_BYTES);
  const srd_t rW0 = mk(is_q ? wqB : woA, W_BYTES), rW1 = mk(is_q ? wqA : woB, W_BYTES), rOUT = mk(out, 896);
  const bool slow = ((((unsigned)(uintptr_t)wqB >> 7) + (jq >> 1)) & 7) == 3;
  const unsigned pre = is_y ? PREY : is_q ? (slow ? PRES : PREQ) : PREN;
  asm volatile(
    "s_mov_b32 s72, 0x400000\n\t"
    "s_buffer_load_dwordx2 s[54:55], s[24:27], s72\n\t"
    "s_mov_b32 s58, 0\n\t"
    "s_mov_b32 s59, 8\n\t"
    "s_mov_b32 s63, 65536\n\t"
    "s_lshl_b32 s83, s50, 15\n\t"
    "s_mov_b32 s82, 56\n\t"
    "s_mov_b32 s84, s50\n\t"
    "s_cmp_eq_u32 s51, 2\n\t"
    "s_cselect_b64 vcc, 0, -1\n\t"
    "s_add_u32 s73, s90, 0\n\t"
    "s_and_b32 s73, s73, 3\n\t"
    "s_lshl_b32 s73, s73, 10\n\t"
    "s_add_u32 s92, s73, s48\n\t"
    "s_add_u32 s73, s90, 1\n\t"
    "s_and_b32 s73, s73, 3\n\t"
    "s_lshl_b32 s73, s73, 10\n\t"
    "s_add_u32 s93, s73, s48\n\t"
    "s_add_u32 s73, s90, 2\n\t"
    "s_and_b32 s73, s73, 3\n\t"
    "s_lshl_b32 s73, s73, 10\n\t"
    "s_add_u32 s94, s73, s48\n\t"
    "s_add_u32 s73, s90, 3\n\t"
    "s_and_b32 s73, s73, 3\n\t"
    "s_lshl_b32 s73, s73, 10\n\t"
    "s_add_u32 s95, s73, s48\n\t"
    "s_cmp_eq_u32 s50, 0\n\t"
    "s_cbranch_scc0 LM0_%=\n\t"
    "ds_read_b128 v[40:43], v17 offset:2048\n\t"
    "ds_read_b32 v44, v63 offset:1036\n\t"
    "v_mov_b32 v32, -1\n\t"
    "v_mov_b32 v33, -1\n\t"
    "v_mov_b32 v34, -1\n\t"
    "v_mov_b32 v35, -1\n\t"
    "s_waitcnt lgkmcnt(0)\n\t"
    "s_mov_b32 s72, 0x0\n\t"
    "buffer_store_dwordx4 v[32:35], v40, s[24:27], s72 offen sc1\n\t"
    "buffer_store_dwordx4 v[32:35], v41, s[24:27], s72 offen sc1\n\t"
    "s_mov_b32 s72, 0x100000\n\t"
    "buffer_store_dwordx4 v[32:35], v40, s[24:27], s72 offen sc1\n\t"
    "buffer_store_dwordx4 v[32:35], v41, s[24:27], s72 offen sc1\n\t"
    "s_mov_b32 s72, 0x200000\n\t"
    "buffer_store_dwordx4 v[32:35], v40, s[24:27], s72 offen sc1\n\t"
    "buffer_store_dwordx4 v[32:35], v41, s[24:27], s72 offen sc1\n\t"
    "s_mov_b32 s72, 0x300000\n\t"
    "buffer_store_dwordx4 v[32:35], v40, s[24:27], s72 offen sc1\n\t"
    "buffer_store_dwordx4 v[32:35], v41, s[24:27], s72 offen sc1\n\t"
    "buffer_store_dwordx4 v[32:35], v42, s[24:27], 0 offen sc1\n\t"
    "buffer_store_dwordx4 v[32:35], v43, s[24:27], 0 offen sc1\n\t"
    "s_cmp_eq_u32 s51, 1\n\t"
    "s_cbranch_scc1 LM0_%=\n\t"
    "buffer_store_dword v32, v44, s[24:27], 0 offen sc1\n\t"
    "LM0_%=:\n\t"
    "s_add_u32 s72, s48, 0x10000\n\t"
    "buffer_load_dwordx4 a[64:67], v0, s[8:11], s72 offen offset:0 nt\n\t"
    "buffer_load_dwordx4 a[68:71], v0, s[8:11], s72 offen offset:128 nt\n\t"
    "s_add_u32 s74, s89, 0x0\n\t"
    "buffer_load_dwordx2 v[64:65], v62, s[40:43], s74 offen\n\t"
    "buffer_load_dwordx4 a[72:75], v0, s[8:11], s72 offen offset:256 nt\n\t"
    "buffer_load_dwordx4 a[76:79], v0, s[8:11], s72 offen offset:384 nt\n\t"
    "s_add_u32 s74, s89, 0x4000\n\t"
    "buffer_load_dwordx2 v[66:67], v62, s[40:43], s74 offen\n\t"
    "s_add_u32 s72, s48, 0x11000\n\t"
    "buffer_load_dwordx4 a[80:83], v0, s[8:11], s72 offen offset:0 nt\n\t"
    "buffer_load_dwordx4 a[84:87], v0, s[8:11], s72 offen offset:128 nt\n\t"
    "s_add_u32 s74, s89, 0x10000\n\t"
    "buffer_load_dwordx2 v[72:73], v62, s[40:43], s74 offen\n\t"
    "buffer_load_dwordx4 a[88:91], v0, s[8:11], s72 offen offset:256 nt\n\t"
    "buffer_load_dwordx4 a[92:95], v0, s[8:11], s72 offen offset:384 nt\n\t"
    "s_add_u32 s74, s89, 0x14000\n\t"
    "buffer_load_dwordx2 v[74:75], v62, s[40:43], s74 offen\n\t"
    "s_waitcnt lgkmcnt(0)\n\t"
    "s_add_u32 s54, s54, 1\n\t"
    "s_addc_u32 s55, s55, 0\n\t"
    "s_lshl_b32 s72, s50, 2\n\t"
    "s_or_b32 s72, s72, s51\n\t"
    "s_cmp_eq_u32 s72, 2\n\t"
    "s_cbranch_scc0 LM4_%=\n\t"
    "s_waitcnt vmcnt(12)\n\t"
    "s_lshl_b32 s72, s53, 3\n\t"
    "s_add_u32 s72, s72, 0x401000\n\t"
    "s_xor_b32 s73, s55, s39\n\t"
    "v_mov_b32 v32, s54\n\t"
    "v_mov_b32 v33, s73\n\t"
    "s_mov_b64 exec, 1\n\t"
    "buffer_store_dwordx2 v[32:33], off, s[24:27], s72 sc1\n\t"
    "s_mov_b64 exec, -1\n\t"
    "LM4_%=:\n\t"
    "s_waitcnt vmcnt(36)\n\t"
    "s_add_u32 s72, s48, 0x18000\n\t"
    "buffer_load_dwordx4 a[96:99], v0, s[8:11], s72 offen offset:0 nt\n\t"
    "buffer_load_dwordx4 a[100:103], v0, s[8:11], s72 offen offset:128 nt\n\t"
    "s_add_u32 s74, s89, 0x20000\n\t"
    "buffer_load_dwordx2 v[80:81], v62, s[40:43], s74 offen\n\t"
    "buffer_load_dwordx4 a[104:107], v0, s[8:11], s72 offen offset:256 nt\n\t"
    "buffer_load_dwordx4 a[108:111], v0, s[8:11], s72 offen offset:384 nt\n\t"
    "s_add_u32 s74, s89, 0x24000\n\t"
    "buffer_load_dwordx2 v[82:83], v62, s[40:43], s74 offen\n\t"
    "s_add_u32 s72, s48, 0x19000\n\t"
    "buffer_load_dwordx4 a[112:115], v0, s[8:11], s72 offen offset:0 nt\n\t"
    "buffer_load_dwordx4 a[116:119], v0, s[8:11], s72 offen offset:128 nt\n\t"
    "s_add_u32 s74, s89, 0x30000\n\t"
    "buffer_load_dwordx2 v[88:89], v62, s[40:43], s74 offen\n\t"
    "buffer_load_dwordx4 a[120:123], v0, s[8:11], s72 offen offset:256 nt\n\t"
    "buffer_load_dwordx4 a[124:127], v0, s[8:11], s72 offen offset:384 nt\n\t"
    "s_add_u32 s74, s89, 0x34000\n\t"
    "buffer_load_dwordx2 v[90:91], v62, s[40:43], s74 offen\n\t"
    "s_add_u32 s72, s48, 0x20000\n\t"
    "buffer_load_dwordx4 a[128:131], v0, s[8:11], s72 offen offset:0 nt\n\t"
    "buffer_load_dwordx4 a[132:135], v0, s[8:11], s72 offen offset:128 nt\n\t"
    "s_add_u32 s74, s89, 0x80000\n\t"
    "buffer_load_dwordx2 v[68:69], v62, s[40:43], s74 offen\n\t"
    "buffer_load_dwordx4 a[136:139], v0, s[8:11], s72 offen offset:256 nt\n\t"
    "buffer_load_dwordx4 a[140:143], v0, s[8:11], s72 offen offset:384 nt\n\t"
    "s_add_u32 s74, s89, 0x84000\n\t"
    "buffer_load_dwordx2 v[70:71], v62, s[40:43], s74 offen\n\t"
    "s_add_u32 s72, s48, 0x21000\n\t"
    "buffer_load_dwordx4 a[144:147], v0, s[8:11], s72 offen offset:0 nt\n\t"
    "buffer_load_dwordx4 a[148:151], v0, s[8:11], s72 offen offset:128 nt\n\t"
    "s_add_u32 s74, s89, 0x90000\n\t"
    "buffer_load_dwordx2 v[76:77], v62, s[40:43], s74 offen\n\t"
    "buffer_load_dwordx4 a[152:155], v0, s[8:11], s72 offen offset:256 nt\n\t"
    "buffer_load_dwordx4 a[156:159], v0, s[8:11], s72 offen offset:384 nt\n\t"
    "s_add_u32 s74, s89, 0x94000\n\t"
    "buffer_load_dwordx2 v[78:79], v62, s[40:43], s74 offen\n\t"
    "s_lshl_b32 s72, s50, 2\n\t"
    "s_or_b32 s72, s72, s51\n\t"
    "s_cmp_lt_u32 s72, 2\n\t"
    "s_cbranch_scc0 LM2_%=\n\t"
    "s_waitcnt vmcnt(24)\n\t"
    "s_lshl_b32 s72, s53, 3\n\t"
    "s_add_u32 s72, s72, 0x401000\n\t"
    "s_xor_b32 s73, s55, s39\n\t"
    "v_mov_b32 v32, s54\n\t"
    "v_mov_b32 v33, s73\n\t"
    "s_mov_b64 exec, 1\n\t"
    "buffer_store_dwordx2 v[32:33], off, s[24:27], s72 sc1\n\t"
    "s_mov_b64 exec, -1\n\t"
    "LM2_%=:\n\t"
    "s_waitcnt vmcnt(36)\n\t"
    "s_add_u32 s72, s48, 0x28000\n\t"
    "buffer_load_dwordx4 a[160:163], v0, s[8:11], s72 offen offset:0 nt\n\t"
    "buffer_load_dwordx4 a[164:167], v0, s[8:11], s72 offen offset:128 nt\n\t"
    "s_add_u32 s74, s89, 0xa0000\n\t"
    "buffer_load_dwordx2 v[84:85], v62, s[40:43], s74 offen\n\t"
    "buffer_load_dwordx4 a[168:171], v0, s[8:11], s72 offen offset:256 nt\n\t"
    "buffer_load_dwordx4 a[172:175], v0, s[8:11], s72 offen offset:384 nt\n\t"
    "s_add_u32 s74, s89, 0xa4000\n\t"
    "buffer_load_dwordx2 v[86:87], v62, s[40:43], s74 offen\n\t"
    "s_add_u32 s72, s48, 0x29000\n\t"
    "buffer_load_dwordx4 a[176:179], v0, s[8:11], s72 offen offset:0 nt\n\t"
    "buffer_load_dwordx4 a[180:183], v0, s[8:11], s72 offen offset:128 nt\n\t"
    "s_add_u32 s74, s89, 0xb0000\n\t"
    "buffer_load_dwordx2 v[92:93], v62, s[40:43], s74 offen\n\t"
    "buffer_load_dwordx4 a[184:187], v0, s[8:11], s72 offen offset:256 nt\n\t"
    "buffer_load_dwordx4 a[188:191], v0, s[8:11], s72 offen offset:384 nt\n\t"
    "s_add_u32 s74, s89, 0xb4000\n\t"
    "buffer_load_dwordx2 v[94:95], v62, s[40:43], s74 offen\n\t"
    "s_add_u32 s72, s48, 0x30000\n\t"
    "buffer_load_dwordx4 a[192:195], v0, s[8:11], s72 offen offset:0 nt\n\t"
    "buffer_load_dwordx4 a[196:199], v0, s[8:11], s72 offen offset:128 nt\n\t"
    "buffer_load_dwordx4 a[200:203], v0, s[8:11], s72 offen offset:256 nt\n\t"
    "buffer_load_dwordx4 a[204:207], v0, s[8:11], s72 offen offset:384 nt\n\t"
    "s_add_u32 s72, s48, 0x31000\n\t"
    "buffer_load_dwordx4 a[208:211], v0, s[8:11], s72 offen offset:0 nt\n\t"
    "buffer_load_dwordx4 a[212:215], v0, s[8:11], s72 offen offset:128 nt\n\t"
    "buffer_load_dwordx4 a[216:219], v0, s[8:11], s72 offen offset:256 nt\n\t"
    "buffer_load_dwordx4 a[220:223], v0, s[8:11], s72 offen offset:384 nt\n\t"
    "s_waitcnt vmcnt(36)\n\t"
    "s_add_u32 s72, s48, 0x38000\n\t"
    "buffer_load_dwordx4 a[224:227], v0, s[8:11], s72 offen offset:0 nt\n\t"
    "buffer_load_dwordx4 a[228:231], v0, s[8:11], s72 offen offset:128 nt\n\t"
    "buffer_load_dwordx4 a[232:235], v0, s[8:11], s72 offen offset:256 nt\n\t"
    "buffer_load_dwordx4 a[236:239], v0, s[8:11], s72 offen offset:384 nt\n\t"
    "s_add_u32 s72, s48, 0x39000\n\t"
    "buffer_load_dwordx4 a[240:243], v0, s[8:11], s72 offen offset:0 nt\n\t"
    "buffer_load_dwordx4 a[244:247], v0, s[8:11], s72 offen offset:128 nt\n\t"
    "buffer_load_dwordx4 a[248:251], v0, s[8:11], s72 offen offset:256 nt\n\t"
    "buffer_load_dwordx4 a[252:255], v0, s[8:11], s72 offen offset:384 nt\n\t"
    "s_waitcnt vmcnt(16)\n\t"
    "v_lshlrev_b32 v36, 16, v254\n\t"
    "v_and_b32 v37, 0xffff0000, v254\n\t"
    "v_lshlrev_b32 v38, 16, v255\n\t"
    "v_and_b32 v39, 0xffff0000, v255\n\t"
    "ds_write_b128 v17, v[36:39] offset:2048\n\t"
    "s_cmp_eq_u32 s51, 1\n\t"
    "s_cbranch_scc1 LM8_%=\n\t"
    "ds_read_b128 v[18:21], v63\n\t"
    "ds_read_b128 v[22:25], v63 offset:1024\n\t"
    "s_mov_b64 vcc, -1\n\t"
    "s_mov_b32 s60, 1\n\t"
    "s_call_b64 s[64:65], LCQ_%=\n\t"
    "LM8_%=:\n\t"
    "s_cmp_eq_u32 s51, 0\n\t"
    "s_cbranch_scc0 LM9_%=\n\t"
    "buffer_load_dwordx4 v[64:67], v16, s[28:31], s52 offen offset:0\n\t"
    "buffer_load_dwordx4 v[68:71], v16, s[28:31], s52 offen offset:3584\n\t"
    "s_add_u32 s72, s52, 0x3800\n\t"
    "buffer_load_dwordx4 v[72:75], v16, s[28:31], s72 offen offset:0\n\t"
    "buffer_load_dwordx4 v[76:79], v16, s[28:31], s72 offen offset:3584\n\t"
    "s_add_u32 s72, s52, 0x7000\n\t"
    "buffer_load_dwordx4 v[80:83], v16, s[28:31], s72 offen offset:0\n\t"
    "buffer_load_dwordx4 v[84:87], v16, s[28:31], s72 offen offset:3584\n\t"
    "s_add_u32 s72, s52, 0xa800\n\t"
    "buffer_load_dwordx4 v[88:91], v16, s[28:31], s72 offen offset:0\n\t"
    "buffer_load_dwordx4 v[92:95], v16, s[28:31], s72 offen offset:3584\n\t"
    "LM9_%=:\n\t"
    "s_add_u32 s96, s92, 0x0\n\t"
    "s_add_u32 s97, s92, 0x8000\n\t"
    "s_add_u32 s98, s93, 0x0\n\t"
    "s_add_u32 s99, s93, 0x8000\n\t"
    "buffer_load_dwordx4 v[96:99], v1, s[12:15], s96 offen offset:0 nt\n\t"
    "buffer_load_dwordx4 v[100:103], v1, s[12:15], s97 offen offset:0 nt\n\t"
    "buffer_load_dwordx4 v[104:107], v1, s[12:15], s96 offen offset:256 nt\n\t"
    "buffer_load_dwordx4 v[108:111], v1, s[12:15], s97 offen offset:256 nt\n\t"
    "buffer_load_dwordx4 v[112:115], v1, s[12:15], s98 offen offset:0 nt\n\t"
    "buffer_load_dwordx4 v[116:119], v1, s[12:15], s99 offen offset:0 nt\n\t"
    "buffer_load_dwordx4 v[120:123], v1, s[12:15], s98 offen offset:256 nt\n\t"
    "buffer_load_dwordx4 v[124:127], v1, s[12:15], s99 offen offset:256 nt\n\t"
    "s_barrier\n\t"
    "s_cmp_eq_u32 s51, 1\n\t"
    "s_cbranch_scc1 LM13_%=\n\t"
    "s_cmp_eq_u32 s50, 0\n\t"
    "s_cbranch_scc0 LM13_%=\n\t"
    "s_mov_b32 s73, 0\n\t"
    "v_mov_b32 v30, 160320\n\t"
    "s_call_b64 s[64:65], LCQF_%=\n\t"
    "LM13_%=:\n\t"
    "s_add_u32 s96, s94, 0x0\n\t"
    "s_add_u32 s97, s94, 0x8000\n\t"
    "s_add_u32 s98, s95, 0x0\n\t"
    "s_add_u32 s99, s95, 0x8000\n\t"
    "buffer_load_dwordx4 v[128:131], v1, s[12:15], s96 offen offset:0 nt\n\t"
    "buffer_load_dwordx4 v[132:135], v1, s[12:15], s97 offen offset:0 nt\n\t"
    "buffer_load_dwordx4 v[136:139], v1, s[12:15], s96 offen offset:256 nt\n\t"
    "buffer_load_dwordx4 v[140:143], v1, s[12:15], s97 offen offset:256 nt\n\t"
    "buffer_load_dwordx4 v[144:147], v1, s[12:15], s98 offen offset:0 nt\n\t"
    "buffer_load_dwordx4 v[148:151], v1, s[12:15], s99 offen offset:0 nt\n\t"
    "buffer_load_dwordx4 v[152:155], v1, s[12:15], s98 offen offset:256 nt\n\t"
    "buffer_load_dwordx4 v[156:159], v1, s[12:15], s99 offen offset:256 nt\n\t"
    "s_mov_b32 s59, 0\n\t"
    "LPB_%=:\n\t"
    "s_cmp_ge_u32 s59, s91\n\t"
    "s_cbranch_scc1 LPBD_%=\n\t"
    "s_waitcnt vmcnt(40)\n\t"
    "s_call_b64 s[66:67], LBB_%=\n\t"
    "s_branch LPB_%=\n\t"
    "LPBD_%=:\n\t"
    "s_waitcnt vmcnt(36)\n\t"
    "v_and_b32 v30, 0x3ff, v2\n\t"
    "v_add_u32 v30, 0x401000, v30\n\t"
    "buffer_load_dwordx4 v[32:35], v30, s[24:27], 0 offen sc1\n\t"
    "buffer_load_dwordx4 v[36:39], v30, s[24:27], 0 offen offset:1024 sc1\n\t"
    "s_add_u32 s96, s94, 0x20000\n\t"
    "s_add_u32 s97, s94, 0x28000\n\t"
    "s_add_u32 s98, s95, 0x20000\n\t"
    "s_add_u32 s99, s95, 0x28000\n\t"
    "buffer_load_dwordx4 v[160:163], v1, s[12:15], s96 offen offset:0\n\t"
    "buffer_load_dwordx4 v[164:167], v1, s[12:15], s97 offen offset:0\n\t"
    "buffer_load_dwordx4 v[168:171], v1, s[12:15], s96 offen offset:256\n\t"
    "buffer_load_dwordx4 v[172:175], v1, s[12:15], s97 offen offset:256\n\t"
    "buffer_load_dwordx4 v[176:179], v1, s[12:15], s98 offen offset:0\n\t"
    "buffer_load_dwordx4 v[180:183], v1, s[12:15], s99 offen offset:0\n\t"
    "buffer_load_dwordx4 v[184:187], v1, s[12:15], s98 offen offset:256\n\t"
    "buffer_load_dwordx4 v[188:191], v1, s[12:15], s99 offen offset:256\n\t"
    "s_add_u32 s96, s92, 0x30000\n\t"
    "s_add_u32 s97, s92, 0x38000\n\t"
    "s_add_u32 s98, s93, 0x30000\n\t"
    "s_add_u32 s99, s93, 0x38000\n\t"
    "buffer_load_dwordx4 v[192:195], v1, s[12:15], s96 offen offset:0\n\t"
    "buffer_load_dwordx4 v[196:199], v1, s[12:15], s97 offen offset:0\n\t"
    "buffer_load_dwordx4 v[200:203], v1, s[12:15], s96 offen offset:256\n\t"
    "buffer_load_dwordx4 v[204:207], v1, s[12:15], s97 offen offset:256\n\t"
    "s_waitcnt vmcnt(12)\n\t"
    "LRD_%=:\n\t"
    "v_xor_b32 v32, s54, v32\n\t"
    "v_xor_b32 v33, s55, v33\n\t"
    "v_xor_b32 v34, s54, v34\n\t"
    "v_xor_b32 v35, s55, v35\n\t"
    "v_xor_b32 v36, s54, v36\n\t"
    "v_xor_b32 v37, s55, v37\n\t"
    "v_xor_b32 v38, s54, v38\n\t"
    "v_xor_b32 v39, s55, v39\n\t"
    "v_and_b32 v31, 0x3fffffff, v37\n\t"
    "v_or3_b32 v32, v32, v33, v34\n\t"
    "v_or3_b32 v32, v32, v35, v36\n\t"
    "v_or3_b32 v32, v32, v31, v38\n\t"
    "v_or_b32 v32, v32, v39\n\t"
    "v_cmp_ne_u32 vcc, 0, v32\n\t"
    "s_cbranch_vccz LRDD_%=\n\t"
    "buffer_load_dwordx4 v[32:35], v30, s[24:27], 0 offen sc1\n\t"
    "buffer_load_dwordx4 v[36:39], v30, s[24:27], 0 offen offset:1024 sc1\n\t"
    "s_waitcnt vmcnt(0)\n\t"
    "s_sub_u32 s63, s63, 1\n\t"
    "s_cbranch_scc0 LRD_%=\n\t"
    "s_mov_b32 s63, 0\n\t"
    "LRDD_%=:\n\t"
    "s_getreg_b32 s75, hwreg(HW_REG_XCC_ID, 2, 1)\n\t"
    "s_lshl_b32 s75, s75, 13\n\t"
    "v_cmp_gt_i32 vcc, 0, v37\n\t"
    "v_lshlrev_b32 v37, 1, v37\n\t"
    "s_bcnt1_i32_b64 s74, vcc\n\t"
    "s_cmp_gt_u32 s74, 8\n\t"
    "s_cselect_b32 s38, 0x2000, 0\n\t"
    "s_xor_b32 s38, s38, s75\n\t"
    "s_mov_b32 s42, s74\n\t"
    "v_cmp_gt_i32 vcc, 0, v37\n\t"
    "s_nop 0\n\t"
    "s_bcnt1_i32_b64 s74, vcc\n\t"
    "s_cmp_gt_u32 s74, 8\n\t"
    "s_cselect_b32 s58, 0x2000, 0\n\t"
    "s_xor_b32 s58, s58, s75\n\t"
    "s_mov_b32 s43, s74\n\t"
    "s_or_b32 s74, s53, s50\n\t"
    "s_cmp_eq_u32 s74, 0\n\t"
    "s_cbranch_scc0 LM3_%=\n\t"
    "s_mov_b32 s74, 0x400000\n\t"
    "v_mov_b32 v32, s54\n\t"
    "v_mov_b32 v33, s55\n\t"
    "s_mov_b64 exec, 1\n\t"
    "buffer_store_dwordx2 v[32:33], off, s[24:27], s74 sc1\n\t"
    "s_mov_b64 exec, -1\n\t"
    "LM3_%=:\n\t"
    "buffer_load_dwordx4 v[48:51], v13, s[24:27], 0 offen sc1\n\t"
    "buffer_load_dwordx4 v[208:211], v1, s[12:15], s98 offen offset:0\n\t"
    "buffer_load_dwordx4 v[212:215], v1, s[12:15], s99 offen offset:0\n\t"
    "buffer_load_dwordx4 v[216:219], v1, s[12:15], s98 offen offset:256\n\t"
    "buffer_load_dwordx4 v[220:223], v1, s[12:15], s99 offen offset:256\n\t"
    "s_add_u32 s96, s94, 0x30000\n\t"
    "s_add_u32 s97, s94, 0x38000\n\t"
    "s_add_u32 s98, s95, 0x30000\n\t"
    "s_add_u32 s99, s95, 0x38000\n\t"
    "buffer_load_dwordx4 v[224:227], v1, s[12:15], s96 offen offset:0\n\t"
    "buffer_load_dwordx4 v[228:231], v1, s[12:15], s97 offen offset:0\n\t"
    "buffer_load_dwordx4 v[232:235], v1, s[12:15], s96 offen offset:256\n\t"
    "buffer_load_dwordx4 v[236:239], v1, s[12:15], s97 offen offset:256\n\t"
    "buffer_load_dwordx4 v[240:243], v1, s[12:15], s98 offen offset:0\n\t"
    "buffer_load_dwordx4 v[244:247], v1, s[12:15], s99 offen offset:0\n\t"
    "buffer_load_dwordx4 v[248:251], v1, s[12:15], s98 offen offset:256\n\t"
    "buffer_load_dwordx4 v[252:255], v1, s[12:15], s99 offen offset:256\n\t"
    "s_waitcnt vmcnt(12)\n\t"
    "s_mov_b32 s56, 0x0\n\t"
    "s_mov_b32 s57, 0x0\n\t"
    "s_mov_b32 s72, 0\n\t"
    "s_mov_b32 s60, 8\n\t"
    "s_call_b64 s[66:67], LPQC_%=\n\t"
    "s_mov_b64 vcc, 0\n\t"
    "s_call_b64 s[64:65], LATT_%=\n\t"
    "s_cmp_eq_u32 s51, 0\n\t"
    "s_cbranch_scc0 LA0N_%=\n\t"
    "s_call_b64 s[64:65], LCY_%=\n\t"
    "s_branch LA0E_%=\n\t"
    "LA0N_%=:\n\t"
    "s_cbranch_execz LNB1_%=\n\t"
    "buffer_store_dword v31, v9, s[24:27], s85 offen sc1\n\t"
    "LNB1_%=:\n\t"
    "s_cmp_eq_u32 s51, 1\n\t"
    "s_cbranch_scc0 LA0E_%=\n\t"
    "LQB_%=:\n\t"
    "s_cmp_ge_u32 s59, 8\n\t"
    "s_cbranch_scc1 LQBD_%=\n\t"
    "s_waitcnt vmcnt(40)\n\t"
    "s_call_b64 s[66:67], LBB_%=\n\t"
    "s_branch LQB_%=\n\t"
    "LQBD_%=:\n\t"
    "s_call_b64 s[64:65], LCQ_%=\n\t"
    "LA0E_%=:\n\t"
    "LMB_%=:\n\t"
    "s_cmp_ge_u32 s59, 8\n\t"
    "s_cbranch_scc1 LMBD_%=\n\t"
    "s_waitcnt vmcnt(40)\n\t"
    "s_call_b64 s[66:67], LBB_%=\n\t"
    "s_branch LMB_%=\n\t"
    "LMBD_%=:\n\t"
    "s_waitcnt vmcnt(4)\n\t"
    "LSEG1_%=:\n\t"
    "s_mov_b32 s56, 0x100000\n\t"
    "s_mov_b32 s57, 0x10000\n\t"
    "s_mov_b32 s60, 32\n\t"
    "s_call_b64 s[66:67], LPQ_%=\n\t"
    "s_cbranch_execz LNB2_%=\n\t"
    "s_cmp_eq_u32 s51, 0\n\t"
    "s_cbranch_scc0 LM11_%=\n\t"
    "buffer_load_dwordx4 v[64:67], v16, s[32:35], s52 offen offset:0 nt\n\t"
    "buffer_load_dwordx4 v[68:71], v16, s[32:35], s52 offen offset:3584 nt\n\t"
    "s_add_u32 s72, s52, 0x3800\n\t"
    "buffer_load_dwordx4 v[72:75], v16, s[32:35], s72 offen offset:0 nt\n\t"
    "buffer_load_dwordx4 v[76:79], v16, s[32:35], s72 offen offset:3584 nt\n\t"
    "s_add_u32 s72, s52, 0x7000\n\t"
    "buffer_load_dwordx4 v[80:83], v16, s[32:35], s72 offen offset:0 nt\n\t"
    "buffer_load_dwordx4 v[84:87], v16, s[32:35], s72 offen offset:3584 nt\n\t"
    "s_add_u32 s72, s52, 0xa800\n\t"
    "buffer_load_dwordx4 v[88:91], v16, s[32:35], s72 offen offset:0 nt\n\t"
    "buffer_load_dwordx4 v[92:95], v16, s[32:35], s72 offen offset:3584 nt\n\t"
    "LM11_%=:\n\t"
    "LNB2_%=:\n\t"
    "s_call_b64 s[64:65], LATTL_%=\n\t"
    "s_cmp_eq_u32 s51, 0\n\t"
    "s_cbranch_scc0 LA1N_%=\n\t"
    "s_call_b64 s[64:65], LCY_%=\n\t"
    "s_branch LA1E_%=\n\t"
    "LA1N_%=:\n\t"
    "s_cbranch_execz LNB4_%=\n\t"
    "buffer_store_dword v31, v9, s[24:27], s85 offen sc1\n\t"
    "LNB4_%=:\n\t"
    "s_cmp_eq_u32 s51, 1\n\t"
    "s_cbranch_scc0 LA1E_%=\n\t"
    "s_mov_b64 s[68:69], s[32:33]\n\t"
    "s_mov_b64 s[70:71], s[34:35]\n\t"
    "s_cbranch_execz LNB3_%=\n\t"
    "s_call_b64 s[66:67], LWLQ_%=\n\t"
    "LNB3_%=:\n\t"
    "s_call_b64 s[64:65], LCQ_%=\n\t"
    "LA1E_%=:\n\t"
    "s_add_u32 s96, s94, 0x20000\n\t"
    "s_add_u32 s97, s94, 0x28000\n\t"
    "s_add_u32 s98, s95, 0x20000\n\t"
    "s_add_u32 s99, s95, 0x28000\n\t"
    "s_cbranch_execz LSRET_%=\n\t"
    "buffer_load_dwordx4 v[64:67], v1, s[12:15], s96 offen offset:0\n\t"
    "buffer_load_dwordx4 v[68:71], v1, s[12:15], s97 offen offset:0\n\t"
    "buffer_load_dwordx4 v[72:75], v1, s[12:15], s96 offen offset:256\n\t"
    "buffer_load_dwordx4 v[76:79], v1, s[12:15], s97 offen offset:256\n\t"
    "buffer_load_dwordx4 v[80:83], v1, s[12:15], s98 offen offset:0\n\t"
    "buffer_load_dwordx4 v[84:87], v1, s[12:15], s99 offen offset:0\n\t"
    "buffer_load_dwordx4 v[88:91], v1, s[12:15], s98 offen offset:256\n\t"
    "buffer_load_dwordx4 v[92:95], v1, s[12:15], s99 offen offset:256\n\t"
    "LSEG2_%=:\n\t"
    "s_mov_b32 s38, s58\n\t"
    "s_mov_b32 s56, 0x200000\n\t"
    "s_mov_b32 s57, 0x20000\n\t"
    "s_mov_b32 s60, 2\n\t"
    "s_call_b64 s[66:67], LPQ_%=\n\t"
    "s_mov_b64 vcc, exec\n\t"
    "s_call_b64 s[64:65], LATT_%=\n\t"
    "s_cmp_eq_u32 s51, 0\n\t"
    "s_cbranch_scc0 LA2N_%=\n\t"
    "s_call_b64 s[64:65], LCY_%=\n\t"
    "s_branch LA2E_%=\n\t"
    "LA2N_%=:\n\t"
    "s_cbranch_execz LNB6_%=\n\t"
    "buffer_store_dword v31, v9, s[24:27], s85 offen sc1\n\t"
    "LNB6_%=:\n\t"
    "s_cmp_eq_u32 s51, 1\n\t"
    "s_cbranch_scc0 LA2E_%=\n\t"
    "s_mov_b64 s[68:69], s[28:29]\n\t"
    "s_mov_b64 s[70:71], s[30:31]\n\t"
    "s_cbranch_execz LNB5_%=\n\t"
    "s_call_b64 s[66:67], LWLQ_%=\n\t"
    "LNB5_%=:\n\t"
    "s_call_b64 s[64:65], LCQ_%=\n\t"
    "LA2E_%=:\n\t"
    "s_cbranch_execz LNB7_%=\n\t"
    "s_cmp_eq_u32 s51, 0\n\t"
    "s_cbranch_scc0 LM6_%=\n\t"
    "s_mov_b64 s[68:69], s[32:33]\n\t"
    "s_mov_b64 s[70:71], s[34:35]\n\t"
    "s_call_b64 s[66:67], LWLY_%=\n\t"
    "LM6_%=:\n\t"
    "LNB7_%=:\n\t"
    "ds_read_b128 a[0:3], v2 offset:0\n\t"
    "ds_read_b128 a[4:7], v2 offset:1024\n\t"
    "ds_read_b128 a[8:11], v2 offset:2048\n\t"
    "ds_read_b128 a[12:15], v2 offset:3072\n\t"
    "ds_read_b128 a[16:19], v2 offset:4096\n\t"
    "ds_read_b128 a[20:23], v2 offset:5120\n\t"
    "ds_read_b128 a[24:27], v2 offset:6144\n\t"
    "ds_read_b128 a[28:31], v2 offset:7168\n\t"
    "s_waitcnt lgkmcnt(4)\n\t"
    "ds_read_b128 a[32:35], v2 offset:8192\n\t"
    "ds_read_b128 a[36:39], v2 offset:9216\n\t"
    "ds_read_b128 a[40:43], v2 offset:10240\n\t"
    "ds_read_b128 a[44:47], v2 offset:11264\n\t"
    "ds_read_b128 a[48:51], v2 offset:12288\n\t"
    "ds_read_b128 a[52:55], v2 offset:13312\n\t"
    "ds_read_b128 a[56:59], v2 offset:14336\n\t"
    "ds_read_b128 a[60:63], v2 offset:15360\n\t"
    "s_waitcnt lgkmcnt(4)\n\t"
    "ds_read_b128 v[96:99], v2 offset:16384\n\t"
    "ds_read_b128 v[100:103], v2 offset:17408\n\t"
    "ds_read_b128 v[104:107], v2 offset:18432\n\t"
    "ds_read_b128 v[108:111], v2 offset:19456\n\t"
    "ds_read_b128 v[112:115], v2 offset:20480\n\t"
    "ds_read_b128 v[116:119], v2 offset:21504\n\t"
    "ds_read_b128 v[120:123], v2 offset:22528\n\t"
    "ds_read_b128 v[124:127], v2 offset:23552\n\t"
    "s_waitcnt lgkmcnt(4)\n\t"
    "ds_read_b128 v[128:131], v2 offset:24576\n\t"
    "ds_read_b128 v[132:135], v2 offset:25600\n\t"
    "ds_read_b128 v[136:139], v2 offset:26624\n\t"
    "ds_read_b128 v[140:143], v2 offset:27648\n\t"
    "ds_read_b128 v[144:147], v2 offset:28672\n\t"
    "ds_read_b128 v[148:151], v2 offset:29696\n\t"
    "ds_read_b128 v[152:155], v2 offset:30720\n\t"
    "ds_read_b128 v[156:159], v2 offset:31744\n\t"
    "s_waitcnt lgkmcnt(0)\n\t"
    "s_cbranch_execz LSRET_%=\n\t"
    "LSEG3_%=:\n\t"
    "s_mov_b32 s56, 0x300000\n\t"
    "s_mov_b32 s57, 0x30000\n\t"
    "s_mov_b32 s60, 20\n\t"
    "s_call_b64 s[66:67], LPQ_%=\n\t"
    "s_mov_b64 vcc, 0\n\t"
    "s_call_b64 s[64:65], LATT_%=\n\t"
    "s_cmp_eq_u32 s51, 0\n\t"
    "s_cbranch_scc0 LA3N_%=\n\t"
    "s_call_b64 s[64:65], LCY_%=\n\t"
    "s_branch LA3E_%=\n\t"
    "LA3N_%=:\n\t"
    "s_cbranch_execz LNB8_%=\n\t"
    "buffer_store_dword v31, v9, s[24:27], s85 offen sc1\n\t"
    "LNB8_%=:\n\t"
    "s_cmp_eq_u32 s51, 1\n\t"
    "s_cbranch_scc0 LA3E_%=\n\t"
    "s_cmp_eq_u32 s53, 1\n\t"
    "s_cbranch_scc0 LA3E_%=\n\t"
    "s_call_b64 s[64:65], LCQ_%=\n\t"
    "LA3E_%=:\n\t"
    "s_cbranch_execz LSRET_%=\n\t"
    "s_branch LEND_%=\n\t"
    "LSRET_%=:\n\t"
    "s_setpc_b64 s[86:87]\n\t"
    "LATT_%=:\n\t"
    "v_mov_b32 v60, 0xff800000\n\t"
    "v_mov_b32 v61, 0xff800000\n\t"
    "s_waitcnt lgkmcnt(3)\n\t"
    "v_mfma_f32_16x16x32_bf16 v[48:51], v[32:35], a[0:3], 0\n\t"
    "v_mov_b32 v62, 0xff800000\n\t"
    "s_waitcnt lgkmcnt(2)\n\t"
    "v_mfma_f32_16x16x32_bf16 v[48:51], v[36:39], a[4:7], v[48:51]\n\t"
    "v_mov_b32 v63, 0xff800000\n\t"
    "s_waitcnt lgkmcnt(1)\n\t"
    "v_mfma_f32_16x16x32_bf16 v[48:51], v[40:43], a[8:11], v[48:51]\n\t"
    "s_waitcnt lgkmcnt(0)\n\t"
    "v_mfma_f32_16x16x32_bf16 v[48:51], v[44:47], a[12:15], v[48:51]\n\t"
    "v_mfma_f32_16x16x32_bf16 v[52:55], v[32:35], a[16:19], 0\n\t"
    "s_add_u32 s96, s92, 0x30000\n\t"
    "s_add_u32 s97, s92, 0x38000\n\t"
    "v_mfma_f32_16x16x32_bf16 v[52:55], v[36:39], a[20:23], v[52:55]\n\t"
    "s_add_u32 s98, s93, 0x30000\n\t"
    "s_add_u32 s99, s93, 0x38000\n\t"
    "v_mfma_f32_16x16x32_bf16 v[52:55], v[40:43], a[24:27], v[52:55]\n\t"
    "s_add_u32 s74, s94, 0x30000\n\t"
    "s_add_u32 s75, s94, 0x38000\n\t"
    "v_mfma_f32_16x16x32_bf16 v[52:55], v[44:47], a[28:31], v[52:55]\n\t"
    "s_add_u32 s76, s95, 0x30000\n\t"
    "s_add_u32 s77, s95, 0x38000\n\t"
    "v_mfma_f32_16x16x32_bf16 v[56:59], v[32:35], a[32:35], 0\n\t"
    "v_mfma_f32_16x16x32_bf16 v[56:59], v[36:39], a[36:39], v[56:59]\n\t"
    "v_add_f32_dpp v60, v50, v48 row_ror:8 row_mask:0x1 bank_mask:0x3\n\t"
    "v_add_f32_dpp v61, v51, v49 row_ror:8 row_mask:0x1 bank_mask:0x3\n\t"
    "v_mfma_f32_16x16x32_bf16 v[56:59], v[40:43], a[40:43], v[56:59]\n\t"
    "v_mfma_f32_16x16x32_bf16 v[56:59], v[44:47], a[44:47], v[56:59]\n\t"
    "v_mfma_f32_16x16x32_bf16 v[48:51], v[32:35], a[48:51], 0\n\t"
    "v_mfma_f32_16x16x32_bf16 v[48:51], v[36:39], a[52:55], v[48:51]\n\t"
    "v_add_f32_dpp v60, v54, v52 row_ror:8 row_mask:0x2 bank_mask:0x3\n\t"
    "v_add_f32_dpp v61, v55, v53 row_ror:8 row_mask:0x2 bank_mask:0x3\n\t"
    "v_mfma_f32_16x16x32_bf16 v[48:51], v[40:43], a[56:59], v[48:51]\n\t"
    "s_bitcmp1_b32 s60, 2\n\t"
    "v_mfma_f32_16x16x32_bf16 v[48:51], v[44:47], a[60:63], v[48:51]\n\t"
    "s_cbranch_scc1 LQKTB_%=\n\t"
    "v_mfma_f32_16x16x32_bf16 v[52:55], v[32:35], a[64:67], 0\n\t"
    "v_mfma_f32_16x16x32_bf16 v[52:55], v[36:39], a[68:71], v[52:55]\n\t"
    "v_add_f32_dpp v60, v58, v56 row_ror:8 row_mask:0x4 bank_mask:0x3\n\t"
    "v_add_f32_dpp v61, v59, v57 row_ror:8 row_mask:0x4 bank_mask:0x3\n\t"
    "v_mfma_f32_16x16x32_bf16 v[52:55], v[40:43], a[72:75], v[52:55]\n\t"
    "s_cbranch_vccz LNR4_%=\n\t"
    "buffer_load_dwordx4 a[0:3], v1, s[12:15], s96 offen offset:0\n\t"
    "buffer_load_dwordx4 a[4:7], v1, s[12:15], s97 offen offset:0\n\t"
    "LNR4_%=:\n\t"
    "v_mfma_f32_16x16x32_bf16 v[52:55], v[44:47], a[76:79], v[52:55]\n\t"
    "v_mfma_f32_16x16x32_bf16 v[56:59], v[32:35], a[80:83], 0\n\t"
    "v_mfma_f32_16x16x32_bf16 v[56:59], v[36:39], a[84:87], v[56:59]\n\t"
    "v_add_f32_dpp v60, v50, v48 row_ror:8 row_mask:0x8 bank_mask:0x3\n\t"
    "v_add_f32_dpp v61, v51, v49 row_ror:8 row_mask:0x8 bank_mask:0x3\n\t"
    "v_mfma_f32_16x16x32_bf16 v[56:59], v[40:43], a[88:91], v[56:59]\n\t"
    "s_cbranch_vccz LNR5_%=\n\t"
    "buffer_load_dwordx4 a[8:11], v1, s[12:15], s96 offen offset:256\n\t"
    "buffer_load_dwordx4 a[12:15], v1, s[12:15], s97 offen offset:256\n\t"
    "LNR5_%=:\n\t"
    "v_mfma_f32_16x16x32_bf16 v[56:59], v[44:47], a[92:95], v[56:59]\n\t"
    "v_mfma_f32_16x16x32_bf16 v[48:51], v[32:35], a[96:99], 0\n\t"
    "v_mfma_f32_16x16x32_bf16 v[48:51], v[36:39], a[100:103], v[48:51]\n\t"
    "v_add_f32_dpp v60, v52, v54 row_ror:8 row_mask:0x1 bank_mask:0xc\n\t"
    "v_add_f32_dpp v61, v53, v55 row_ror:8 row_mask:0x1 bank_mask:0xc\n\t"
    "v_mfma_f32_16x16x32_bf16 v[48:51], v[40:43], a[104:107], v[48:51]\n\t"
    "s_cbranch_vccz LNR6_%=\n\t"
    "buffer_load_dwordx4 a[16:19], v1, s[12:15], s98 offen offset:0\n\t"
    "buffer_load_dwordx4 a[20:23], v1, s[12:15], s99 offen offset:0\n\t"
    "LNR6_%=:\n\t"
    "v_mfma_f32_16x16x32_bf16 v[48:51], v[44:47], a[108:111], v[48:51]\n\t"
    "v_mfma_f32_16x16x32_bf16 v[52:55], v[32:35], a[112:115], 0\n\t"
    "v_mfma_f32_16x16x32_bf16 v[52:55], v[36:39], a[116:119], v[52:55]\n\t"
    "v_add_f32_dpp v60, v56, v58 row_ror:8 row_mask:0x2 bank_mask:0xc\n\t"
    "v_add_f32_dpp v61, v57, v59 row_ror:8 row_mask:0x2 bank_mask:0xc\n\t"
    "v_mfma_f32_16x16x32_bf16 v[52:55], v[40:43], a[120:123], v[52:55]\n\t"
    "s_cbranch_vccz LNR7_%=\n\t"
    "buffer_load_dwordx4 a[24:27], v1, s[12:15], s98 offen offset:256\n\t"
    "buffer_load_dwordx4 a[28:31], v1, s[12:15], s99 offen offset:256\n\t"
    "LNR7_%=:\n\t"
    "v_mfma_f32_16x16x32_bf16 v[52:55], v[44:47], a[124:127], v[52:55]\n\t"
    "v_mfma_f32_16x16x32_bf16 v[56:59], v[32:35], a[128:131], 0\n\t"
    "v_mfma_f32_16x16x32_bf16 v[56:59], v[36:39], a[132:135], v[56:59]\n\t"
    "v_add_f32_dpp v60, v48, v50 row_ror:8 row_mask:0x4 bank_mask:0xc\n\t"
    "v_add_f32_dpp v61, v49, v51 row_ror:8 row_mask:0x4 bank_mask:0xc\n\t"
    "v_mfma_f32_16x16x32_bf16 v[56:59], v[40:43], a[136:139], v[56:59]\n\t"
    "s_cbranch_vccz LNR8_%=\n\t"
    "buffer_load_dwordx4 a[32:35], v1, s[12:15], s74 offen offset:0\n\t"
    "LNR8_%=:\n\t"
    "v_mfma_f32_16x16x32_bf16 v[56:59], v[44:47], a[140:143], v[56:59]\n\t"
    "v_mfma_f32_16x16x32_bf16 v[48:51], v[32:35], a[144:147], 0\n\t"
    "v_mfma_f32_16x16x32_bf16 v[48:51], v[36:39], a[148:151], v[48:51]\n\t"
    "v_add_f32_dpp v60, v52, v54 row_ror:8 row_mask:0x8 bank_mask:0xc\n\t"
    "v_add_f32_dpp v61, v53, v55 row_ror:8 row_mask:0x8 bank_mask:0xc\n\t"
    "v_mfma_f32_16x16x32_bf16 v[48:51], v[40:43], a[152:155], v[48:51]\n\t"
    "s_cbranch_vccz LNR9_%=\n\t"
    "buffer_load_dwordx4 a[36:39], v1, s[12:15], s75 offen offset:0\n\t"
    "LNR9_%=:\n\t"
    "v_mfma_f32_16x16x32_bf16 v[48:51], v[44:47], a[156:159], v[48:51]\n\t"
    "v_mfma_f32_16x16x32_bf16 v[52:55], v[32:35], a[160:163], 0\n\t"
    "v_mfma_f32_16x16x32_bf16 v[52:55], v[36:39], a[164:167], v[52:55]\n\t"
    "v_add_f32_dpp v62, v58, v56 row_ror:8 row_mask:0x1 bank_mask:0x3\n\t"
    "v_add_f32_dpp v63, v59, v57 row_ror:8 row_mask:0x1 bank_mask:0x3\n\t"
    "v_mfma_f32_16x16x32_bf16 v[52:55], v[40:43], a[168:171], v[52:55]\n\t"
    "s_cbranch_vccz LNR10_%=\n\t"
    "buffer_load_dwordx4 a[40:43], v1, s[12:15], s74 offen offset:256\n\t"
    "LNR10_%=:\n\t"
    "v_mfma_f32_16x16x32_bf16 v[52:55], v[44:47], a[172:175], v[52:55]\n\t"
    "v_mfma_f32_16x16x32_bf16 v[56:59], v[32:35], a[176:179], 0\n\t"
    "v_mfma_f32_16x16x32_bf16 v[56:59], v[36:39], a[180:183], v[56:59]\n\t"
    "v_add_f32_dpp v62, v50, v48 row_ror:8 row_mask:0x2 bank_mask:0x3\n\t"
    "v_add_f32_dpp v63, v51, v49 row_ror:8 row_mask:0x2 bank_mask:0x3\n\t"
    "v_mfma_f32_16x16x32_bf16 v[56:59], v[40:43], a[184:187], v[56:59]\n\t"
    "s_cbranch_vccz LNR11_%=\n\t"
    "buffer_load_dwordx4 a[44:47], v1, s[12:15], s75 offen offset:256\n\t"
    "LNR11_%=:\n\t"
    "v_mfma_f32_16x16x32_bf16 v[56:59], v[44:47], a[188:191], v[56:59]\n\t"
    "v_mfma_f32_16x16x32_bf16 v[48:51], v[32:35], a[192:195], 0\n\t"
    "v_mfma_f32_16x16x32_bf16 v[48:51], v[36:39], a[196:199], v[48:51]\n\t"
    "v_add_f32_dpp v62, v54, v52 row_ror:8 row_mask:0x4 bank_mask:0x3\n\t"
    "v_add_f32_dpp v63, v55, v53 row_ror:8 row_mask:0x4 bank_mask:0x3\n\t"
    "v_mfma_f32_16x16x32_bf16 v[48:51], v[40:43], a[200:203], v[48:51]\n\t"
    "s_cbranch_vccz LNR12_%=\n\t"
    "buffer_load_dwordx4 a[48:51], v1, s[12:15], s76 offen offset:0\n\t"
    "LNR12_%=:\n\t"
    "v_mfma_f32_16x16x32_bf16 v[48:51], v[44:47], a[204:207], v[48:51]\n\t"
    "v_mfma_f32_16x16x32_bf16 v[52:55], v[32:35], a[208:211], 0\n\t"
    "v_mfma_f32_16x16x32_bf16 v[52:55], v[36:39], a[212:215], v[52:55]\n\t"
    "v_add_f32_dpp v62, v58, v56 row_ror:8 row_mask:0x8 bank_mask:0x3\n\t"
    "v_add_f32_dpp v63, v59, v57 row_ror:8 row_mask:0x8 bank_mask:0x3\n\t"
    "v_mfma_f32_16x16x32_bf16 v[52:55], v[40:43], a[216:219], v[52:55]\n\t"
    "s_cbranch_vccz LNR13_%=\n\t"
    "buffer_load_dwordx4 a[52:55], v1, s[12:15], s77 offen offset:0\n\t"
    "LNR13_%=:\n\t"
    "v_mfma_f32_16x16x32_bf16 v[52:55], v[44:47], a[220:223], v[52:55]\n\t"
    "v_mfma_f32_16x16x32_bf16 v[56:59], v[32:35], a[224:227], 0\n\t"
    "v_mfma_f32_16x16x32_bf16 v[56:59], v[36:39], a[228:231], v[56:59]\n\t"
    "v_add_f32_dpp v62, v48, v50 row_ror:8 row_mask:0x1 bank_mask:0xc\n\t"
    "v_add_f32_dpp v63, v49, v51 row_ror:8 row_mask:0x1 bank_mask:0xc\n\t"
    "v_mfma_f32_16x16x32_bf16 v[56:59], v[40:43], a[232:235], v[56:59]\n\t"
    "s_cbranch_vccz LNR14_%=\n\t"
    "buffer_load_dwordx4 a[56:59], v1, s[12:15], s76 offen offset:256\n\t"
    "LNR14_%=:\n\t"
    "v_mfma_f32_16x16x32_bf16 v[56:59], v[44:47], a[236:239], v[56:59]\n\t"
    "v_mfma_f32_16x16x32_bf16 v[48:51], v[32:35], a[240:243], 0\n\t"
    "v_mfma_f32_16x16x32_bf16 v[48:51], v[36:39], a[244:247], v[48:51]\n\t"
    "v_add_f32_dpp v62, v52, v54 row_ror:8 row_mask:0x2 bank_mask:0xc\n\t"
    "v_add_f32_dpp v63, v53, v55 row_ror:8 row_mask:0x2 bank_mask:0xc\n\t"
    "v_mfma_f32_16x16x32_bf16 v[48:51], v[40:43], a[248:251], v[48:51]\n\t"
    "s_cbranch_vccz LNR15_%=\n\t"
    "buffer_load_dwordx4 a[60:63], v1, s[12:15], s77 offen offset:256\n\t"
    "LNR15_%=:\n\t"
    "v_mfma_f32_16x16x32_bf16 v[48:51], v[44:47], a[252:255], v[48:51]\n\t"
    "s_nop 1\n\t"
    "v_add_f32_dpp v62, v56, v58 row_ror:8 row_mask:0x4 bank_mask:0xc\n\t"
    "v_add_f32_dpp v63, v57, v59 row_ror:8 row_mask:0x4 bank_mask:0xc\n\t"
    "s_nop 3\n\t"
    "v_add_f32_dpp v62, v48, v50 row_ror:8 row_mask:0x8 bank_mask:0xc\n\t"
    "v_add_f32_dpp v63, v49, v51 row_ror:8 row_mask:0x8 bank_mask:0xc\n\t"
    "LSOFT_%=:\n\t"
    "v_max_f32 v30, v60, v62\n\t"
    "v_max_f32 v58, v61, v63\n\t"
    "v_mov_b32 v48, 0\n\t"
    "v_mov_b32 v49, 0\n\t"
    "v_permlane32_swap_b32 v30, v58\n\t"
    "v_max_f32 v30, v30, v58\n\t"
    "v_mov_b32 v58, v30\n\t"
    "v_mov_b32 v50, 0\n\t"
    "v_mov_b32 v51, 0\n\t"
    "v_permlane16_swap_b32 v58, v30\n\t"
    "v_max_f32 v30, v30, v58\n\t"
    "v_mov_b32 v52, 0\n\t"
    "v_mov_b32 v53, 0\n\t"
    "v_max_f32_dpp v30, v30, v30 row_ror:8 row_mask:0xf bank_mask:0xf\n\t"
    "v_mov_b32 v54, 0\n\t"
    "v_mov_b32 v55, 0\n\t"
    "v_max_f32_dpp v30, v30, v30 row_ror:4 row_mask:0xf bank_mask:0xf\n\t"
    "s_nop 1\n\t"
    "v_max_f32_dpp v30, v30, v30 row_ror:2 row_mask:0xf bank_mask:0xf\n\t"
    "s_nop 1\n\t"
    "v_max_f32_dpp v30, v30, v30 row_ror:1 row_mask:0xf bank_mask:0xf\n\t"
    "v_ceil_f32 v30, v30\n\t"
    "s_nop 0\n\t"
    "v_readlane_b32 s61, v30, 0\n\t"
    "v_readlane_b32 s62, v30, 32\n\t"
    "s_bitcmp1_b32 s60, 3\n\t"
    "v_subrev_f32 v60, s61, v60\n\t"
    "v_subrev_f32 v62, s61, v62\n\t"
    "v_subrev_f32 v61, s62, v61\n\t"
    "v_subrev_f32 v63, s62, v63\n\t"
    "v_exp_f32 v60, v60\n\t"
    "v_exp_f32 v61, v61\n\t"
    "v_exp_f32 v62, v62\n\t"
    "v_exp_f32 v63, v63\n\t"
    "v_cvt_pk_bf16_f32 v56, v60, v61\n\t"
    "v_cvt_pk_bf16_f32 v57, v62, v63\n\t"
    "v_add_f32 v31, v60, v62\n\t"
    "v_add_f32 v58, v61, v63\n\t"
    "s_cbranch_scc1 LPVX_%=\n\t"
    "s_and_b32 s72, s60, 34\n\t"
    "s_cbranch_scc1 LPVD_%=\n\t"
    "LPVR0_%=:\n\t"
    "v_and_b32_dpp v32, v56, v3 row_newbcast:0 row_mask:0xf bank_mask:0xf\n\t"
    "v_and_b32_dpp v33, v56, v4 row_newbcast:0 row_mask:0xf bank_mask:0xf\n\t"
    "v_permlane32_swap_b32 v31, v58\n\t"
    "v_add_f32 v31, v31, v58\n\t"
    "v_mov_b32 v58, v31\n\t"
    "v_and_b32_dpp v34, v56, v3 row_newbcast:1 row_mask:0xf bank_mask:0xf\n\t"
    "v_and_b32_dpp v35, v56, v4 row_newbcast:1 row_mask:0xf bank_mask:0xf\n\t"
    "v_permlane16_swap_b32 v58, v31\n\t"
    "v_add_f32 v31, v31, v58\n\t"
    "v_and_b32_dpp v36, v56, v3 row_newbcast:2 row_mask:0xf bank_mask:0xf\n\t"
    "v_and_b32_dpp v37, v56, v4 row_newbcast:2 row_mask:0xf bank_mask:0xf\n\t"
    "v_add_f32_dpp v31, v31, v31 row_ror:8 row_mask:0xf bank_mask:0xf\n\t"
    "v_smfmac_f32_16x16x64_bf16 v[48:51], v[32:35], v[96:103], v5\n\t"
    "v_and_b32_dpp v38, v56, v3 row_newbcast:3 row_mask:0xf bank_mask:0xf\n\t"
    "v_add_f32_dpp v31, v31, v31 row_ror:4 row_mask:0xf bank_mask:0xf\n\t"
    "v_and_b32_dpp v39, v56, v4 row_newbcast:3 row_mask:0xf bank_mask:0xf\n\t"
    "v_smfmac_f32_16x16x64_bf16 v[52:55], v[32:35], v[104:111], v5\n\t"
    "v_add_f32_dpp v31, v31, v31 row_ror:2 row_mask:0xf bank_mask:0xf\n\t"
    "v_smfmac_f32_16x16x64_bf16 v[48:51], v[36:39], v[112:119], v5\n\t"
    "v_and_b32_dpp v32, v56, v3 row_newbcast:4 row_mask:0xf bank_mask:0xf\n\t"
    "v_add_f32_dpp v31, v31, v31 row_ror:1 row_mask:0xf bank_mask:0xf\n\t"
    "v_smfmac_f32_16x16x64_bf16 v[52:55], v[36:39], v[120:127], v5\n\t"
    "ds_write_b64 v12, v[30:31]\n\t"
    "v_and_b32_dpp v33, v56, v4 row_newbcast:4 row_mask:0xf bank_mask:0xf\n\t"
    "v_and_b32_dpp v34, v56, v3 row_newbcast:5 row_mask:0xf bank_mask:0xf\n\t"
    "v_and_b32_dpp v35, v56, v4 row_newbcast:5 row_mask:0xf bank_mask:0xf\n\t"
    "v_and_b32_dpp v36, v56, v3 row_newbcast:6 row_mask:0xf bank_mask:0xf\n\t"
    "v_and_b32_dpp v37, v56, v4 row_newbcast:6 row_mask:0xf bank_mask:0xf\n\t"
    "v_smfmac_f32_16x16x64_bf16 v[48:51], v[32:35], v[128:135], v5\n\t"
    "v_and_b32_dpp v38, v56, v3 row_newbcast:7 row_mask:0xf bank_mask:0xf\n\t"
    "v_and_b32_dpp v39, v56, v4 row_newbcast:7 row_mask:0xf bank_mask:0xf\n\t"
    "s_waitcnt lgkmcnt(0)\n\t"
    "s_cbranch_execz LNB9_%=\n\t"
    "s_barrier\n\t"
    "LNB9_%=:\n\t"
    "ds_read_b64 v[60:61], v11\n\t"
    "v_smfmac_f32_16x16x64_bf16 v[52:55], v[32:35], v[136:143], v5\n\t"
    "v_smfmac_f32_16x16x64_bf16 v[48:51], v[36:39], v[144:151], v5\n\t"
    "v_smfmac_f32_16x16x64_bf16 v[52:55], v[36:39], v[152:159], v5\n\t"
    "s_waitcnt lgkmcnt(0)\n\t"
    "v_max_f32_dpp v62, v60, v60 quad_perm:[1,0,3,2] row_mask:0xf bank_mask:0xf\n\t"
    "s_nop 1\n\t"
    "v_max_f32_dpp v62, v62, v62 quad_perm:[2,3,0,1] row_mask:0xf bank_mask:0xf\n\t"
    "v_sub_f32 v60, v60, v62\n\t"
    "v_sub_f32 v59, v30, v62\n\t"
    "v_cvt_i32_f32 v60, v60\n\t"
    "v_cvt_i32_f32 v59, v59\n\t"
    "v_ldexp_f32 v61, v61, v60\n\t"
    "v_min_u32 v62, -2, v62\n\t"
    "s_nop 0\n\t"
    "v_add_f32_dpp v63, v61, v61 quad_perm:[1,0,3,2] row_mask:0xf bank_mask:0xf\n\t"
    "s_nop 1\n\t"
    "v_add_f32_dpp v63, v63, v63 quad_perm:[2,3,0,1] row_mask:0xf bank_mask:0xf\n\t"
    "v_min_u32 v63, -2, v63\n\t"
    "s_cbranch_execz LNB10_%=\n\t"
    "buffer_store_dwordx2 v[62:63], v10, s[24:27], s56 offen sc1\n\t"
    "LNB10_%=:\n\t"
    "s_bitcmp1_b32 s60, 2\n\t"
    "s_cbranch_scc1 LMERGE_%=\n\t"
    "s_bitcmp1_b32 s60, 3\n\t"
    "s_cbranch_scc1 LHK2_%=\n\t"
    "LHR2_%=:\n\t"
    "v_and_b32_dpp v32, v56, v3 row_newbcast:8 row_mask:0xf bank_mask:0xf\n\t"
    "v_and_b32_dpp v33, v56, v4 row_newbcast:8 row_mask:0xf bank_mask:0xf\n\t"
    "v_and_b32_dpp v34, v56, v3 row_newbcast:9 row_mask:0xf bank_mask:0xf\n\t"
    "v_and_b32_dpp v35, v56, v4 row_newbcast:9 row_mask:0xf bank_mask:0xf\n\t"
    "v_and_b32_dpp v36, v56, v3 row_newbcast:10 row_mask:0xf bank_mask:0xf\n\t"
    "v_and_b32_dpp v37, v56, v4 row_newbcast:10 row_mask:0xf bank_mask:0xf\n\t"
    "v_smfmac_f32_16x16x64_bf16 v[48:51], v[32:35], v[160:167], v5\n\t"
    "v_and_b32_dpp v38, v56, v3 row_newbcast:11 row_mask:0xf bank_mask:0xf\n\t"
    "v_and_b32_dpp v39, v56, v4 row_newbcast:11 row_mask:0xf bank_mask:0xf\n\t"
    "v_smfmac_f32_16x16x64_bf16 v[52:55], v[32:35], v[168:175], v5\n\t"
    "s_nop 0\n\t"
    "v_smfmac_f32_16x16x64_bf16 v[48:51], v[36:39], v[176:183], v5\n\t"
    "v_smfmac_f32_16x16x64_bf16 v[52:55], v[36:39], v[184:191], v5\n\t"
    "s_cbranch_scc1 LHK3_%=\n\t"
    "LHR3_%=:\n\t"
    "v_and_b32_dpp v32, v56, v3 row_newbcast:12 row_mask:0xf bank_mask:0xf\n\t"
    "v_and_b32_dpp v33, v56, v4 row_newbcast:12 row_mask:0xf bank_mask:0xf\n\t"
    "v_and_b32_dpp v34, v56, v3 row_newbcast:13 row_mask:0xf bank_mask:0xf\n\t"
    "v_and_b32_dpp v35, v56, v4 row_newbcast:13 row_mask:0xf bank_mask:0xf\n\t"
    "v_and_b32_dpp v36, v56, v3 row_newbcast:14 row_mask:0xf bank_mask:0xf\n\t"
    "v_and_b32_dpp v37, v56, v4 row_newbcast:14 row_mask:0xf bank_mask:0xf\n\t"
    "v_smfmac_f32_16x16x64_bf16 v[48:51], v[32:35], v[192:199], v5\n\t"
    "v_and_b32_dpp v38, v56, v3 row_newbcast:15 row_mask:0xf bank_mask:0xf\n\t"
    "v_and_b32_dpp v39, v56, v4 row_newbcast:15 row_mask:0xf bank_mask:0xf\n\t"
    "v_smfmac_f32_16x16x64_bf16 v[52:55], v[32:35], v[200:207], v5\n\t"
    "s_nop 0\n\t"
    "v_smfmac_f32_16x16x64_bf16 v[48:51], v[36:39], v[208:215], v5\n\t"
    "v_smfmac_f32_16x16x64_bf16 v[52:55], v[36:39], v[216:223], v5\n\t"
    "s_cbranch_scc1 LHK4_%=\n\t"
    "LHR4_%=:\n\t"
    "v_and_b32_dpp v32, v57, v3 row_newbcast:0 row_mask:0xf bank_mask:0xf\n\t"
    "v_and_b32_dpp v33, v57, v4 row_newbcast:0 row_mask:0xf bank_mask:0xf\n\t"
    "v_and_b32_dpp v34, v57, v3 row_newbcast:1 row_mask:0xf bank_mask:0xf\n\t"
    "v_and_b32_dpp v35, v57, v4 row_newbcast:1 row_mask:0xf bank_mask:0xf\n\t"
    "v_and_b32_dpp v36, v57, v3 row_newbcast:2 row_mask:0xf bank_mask:0xf\n\t"
    "v_and_b32_dpp v37, v57, v4 row_newbcast:2 row_mask:0xf bank_mask:0xf\n\t"
    "v_smfmac_f32_16x16x64_bf16 v[48:51], v[32:35], v[224:231], v5\n\t"
    "v_and_b32_dpp v38, v57, v3 row_newbcast:3 row_mask:0xf bank_mask:0xf\n\t"
    "v_and_b32_dpp v39, v57, v4 row_newbcast:3 row_mask:0xf bank_mask:0xf\n\t"
    "v_smfmac_f32_16x16x64_bf16 v[52:55], v[32:35], v[232:239], v5\n\t"
    "s_nop 0\n\t"
    "v_smfmac_f32_16x16x64_bf16 v[48:51], v[36:39], v[240:247], v5\n\t"
    "v_smfmac_f32_16x16x64_bf16 v[52:55], v[36:39], v[248:255], v5\n\t"
    "s_cbranch_scc1 LMERGE_%=\n\t"
    "s_cmp_eq_u32 s51, 0\n\t"
    "v_and_b32_dpp v32, v57, v3 row_newbcast:8 row_mask:0xf bank_mask:0xf\n\t"
    "v_and_b32_dpp v33, v57, v4 row_newbcast:8 row_mask:0xf bank_mask:0xf\n\t"
    "v_and_b32_dpp v34, v57, v3 row_newbcast:9 row_mask:0xf bank_mask:0xf\n\t"
    "v_and_b32_dpp v35, v57, v4 row_newbcast:9 row_mask:0xf bank_mask:0xf\n\t"
    "v_and_b32_dpp v36, v57, v3 row_newbcast:10 row_mask:0xf bank_mask:0xf\n\t"
    "v_and_b32_dpp v37, v57, v4 row_newbcast:10 row_mask:0xf bank_mask:0xf\n\t"
    "s_cbranch_vccz LNW6_%=\n\t"
    "s_waitcnt vmcnt(17)\n\t"
    "s_cbranch_scc1 LNW6_%=\n\t"
    "s_waitcnt vmcnt(9)\n\t"
    "LNW6_%=:\n\t"
    "v_smfmac_f32_16x16x64_bf16 v[48:51], v[32:35], a[0:7], v5\n\t"
    "v_and_b32_dpp v38, v57, v3 row_newbcast:11 row_mask:0xf bank_mask:0xf\n\t"
    "v_and_b32_dpp v39, v57, v4 row_newbcast:11 row_mask:0xf bank_mask:0xf\n\t"
    "v_smfmac_f32_16x16x64_bf16 v[52:55], v[32:35], a[8:15], v5\n\t"
    "s_nop 0\n\t"
    "v_smfmac_f32_16x16x64_bf16 v[48:51], v[36:39], a[16:23], v5\n\t"
    "v_smfmac_f32_16x16x64_bf16 v[52:55], v[36:39], a[24:31], v5\n\t"
    "v_and_b32_dpp v32, v57, v3 row_newbcast:12 row_mask:0xf bank_mask:0xf\n\t"
    "v_and_b32_dpp v33, v57, v4 row_newbcast:12 row_mask:0xf bank_mask:0xf\n\t"
    "v_and_b32_dpp v34, v57, v3 row_newbcast:13 row_mask:0xf bank_mask:0xf\n\t"
    "v_and_b32_dpp v35, v57, v4 row_newbcast:13 row_mask:0xf bank_mask:0xf\n\t"
    "v_and_b32_dpp v36, v57, v3 row_newbcast:14 row_mask:0xf bank_mask:0xf\n\t"
    "v_and_b32_dpp v37, v57, v4 row_newbcast:14 row_mask:0xf bank_mask:0xf\n\t"
    "s_cbranch_vccz LNW7_%=\n\t"
    "s_waitcnt vmcnt(9)\n\t"
    "s_cbranch_scc1 LNW7_%=\n\t"
    "s_waitcnt vmcnt(1)\n\t"
    "LNW7_%=:\n\t"
    "v_smfmac_f32_16x16x64_bf16 v[48:51], v[32:35], a[32:39], v5\n\t"
    "v_and_b32_dpp v38, v57, v3 row_newbcast:15 row_mask:0xf bank_mask:0xf\n\t"
    "v_and_b32_dpp v39, v57, v4 row_newbcast:15 row_mask:0xf bank_mask:0xf\n\t"
    "v_smfmac_f32_16x16x64_bf16 v[52:55], v[32:35], a[40:47], v5\n\t"
    "s_nop 0\n\t"
    "v_smfmac_f32_16x16x64_bf16 v[48:51], v[36:39], a[48:55], v5\n\t"
    "v_smfmac_f32_16x16x64_bf16 v[52:55], v[36:39], a[56:63], v5\n\t"
    "LMERGE_%=:\n\t"
    "s_add_u32 s72, s56, s38\n\t"
    "s_xor_b32 s85, s72, 0x2000\n\t"
    "ds_read_b128 v[18:21], v17\n\t"
    "ds_read_b128 v[22:25], v17 offset:1024\n\t"
    "s_nop 2\n\t"
    "v_ldexp_f32 v48, v48, v59\n\t"
    "v_ldexp_f32 v49, v49, v59\n\t"
    "v_ldexp_f32 v50, v50, v59\n\t"
    "v_ldexp_f32 v51, v51, v59\n\t"
    "ds_write2st64_b64 v7, v[48:49], v[50:51] offset0:0 offset1:1\n\t"
    "v_ldexp_f32 v52, v52, v59\n\t"
    "v_ldexp_f32 v53, v53, v59\n\t"
    "v_ldexp_f32 v54, v54, v59\n\t"
    "v_ldexp_f32 v55, v55, v59\n\t"
    "ds_write2st64_b64 v7, v[52:53], v[54:55] offset0:2 offset1:3\n\t"
    "s_waitcnt lgkmcnt(0)\n\t"
    "s_cbranch_execz LNB11_%=\n\t"
    "s_barrier\n\t"
    "LNB11_%=:\n\t"
    "ds_read2st64_b64 v[32:35], v8 offset0:0 offset1:4\n\t"
    "ds_read2st64_b64 v[36:39], v8 offset0:8 offset1:12\n\t"
    "s_waitcnt lgkmcnt(1)\n\t"
    "v_pk_add_f32 v[32:33], v[32:33], v[34:35]\n\t"
    "s_waitcnt lgkmcnt(0)\n\t"
    "v_pk_add_f32 v[36:37], v[36:37], v[38:39]\n\t"
    "v_pk_add_f32 v[32:33], v[32:33], v[36:37]\n\t"
    "v_cvt_pk_bf16_f32 v31, v32, v33\n\t"
    "v_min_u32 v31, -2, v31\n\t"
    "s_cbranch_execz LNB12_%=\n\t"
    "buffer_store_dword v31, v9, s[24:27], s72 offen sc1\n\t"
    "LNB12_%=:\n\t"
    "s_setpc_b64 s[64:65]\n\t"
    "LQKTB_%=:\n\t"
    "s_nop 1\n\t"
    "v_add_f32_dpp v60, v58, v56 row_ror:8 row_mask:0x4 bank_mask:0x3\n\t"
    "v_add_f32_dpp v61, v59, v57 row_ror:8 row_mask:0x4 bank_mask:0x3\n\t"
    "s_nop 3\n\t"
    "v_add_f32_dpp v60, v50, v48 row_ror:8 row_mask:0x8 bank_mask:0x3\n\t"
    "v_add_f32_dpp v61, v51, v49 row_ror:8 row_mask:0x8 bank_mask:0x3\n\t"
    "s_branch LSOFT_%=\n\t"
    "LPVD_%=:\n\t"
    "s_bitcmp1_b32 s60, 5\n\t"
    "s_cbranch_scc1 LPVLDS_%=\n\t"
    "v_and_b32_dpp v32, v57, v3 row_newbcast:4 row_mask:0xf bank_mask:0xf\n\t"
    "v_and_b32_dpp v33, v57, v4 row_newbcast:4 row_mask:0xf bank_mask:0xf\n\t"
    "v_and_b32_dpp v34, v57, v3 row_newbcast:5 row_mask:0xf bank_mask:0xf\n\t"
    "v_and_b32_dpp v35, v57, v4 row_newbcast:5 row_mask:0xf bank_mask:0xf\n\t"
    "v_and_b32_dpp v36, v57, v3 row_newbcast:6 row_mask:0xf bank_mask:0xf\n\t"
    "v_and_b32_dpp v37, v57, v4 row_newbcast:6 row_mask:0xf bank_mask:0xf\n\t"
    "v_smfmac_f32_16x16x64_bf16 v[48:51], v[32:35], v[64:71], v5\n\t"
    "v_and_b32_dpp v38, v57, v3 row_newbcast:7 row_mask:0xf bank_mask:0xf\n\t"
    "v_and_b32_dpp v39, v57, v4 row_newbcast:7 row_mask:0xf bank_mask:0xf\n\t"
    "v_smfmac_f32_16x16x64_bf16 v[52:55], v[32:35], v[72:79], v5\n\t"
    "s_cmp_eq_u32 s51, 0\n\t"
    "v_smfmac_f32_16x16x64_bf16 v[48:51], v[36:39], v[80:87], v5\n\t"
    "v_smfmac_f32_16x16x64_bf16 v[52:55], v[36:39], v[88:95], v5\n\t"
    "s_cbranch_scc0 LPVR0_%=\n\t"
    "s_cbranch_execz LPVR0_%=\n\t"
    "buffer_load_dwordx4 v[64:67], v16, s[28:31], s52 offen offset:0 nt\n\t"
    "buffer_load_dwordx4 v[68:71], v16, s[28:31], s52 offen offset:3584 nt\n\t"
    "s_add_u32 s72, s52, 0x3800\n\t"
    "buffer_load_dwordx4 v[72:75], v16, s[28:31], s72 offen offset:0 nt\n\t"
    "buffer_load_dwordx4 v[76:79], v16, s[28:31], s72 offen offset:3584 nt\n\t"
    "s_add_u32 s72, s52, 0x7000\n\t"
    "buffer_load_dwordx4 v[80:83], v16, s[28:31], s72 offen offset:0 nt\n\t"
    "buffer_load_dwordx4 v[84:87], v16, s[28:31], s72 offen offset:3584 nt\n\t"
    "s_add_u32 s72, s52, 0xa800\n\t"
    "buffer_load_dwordx4 v[88:91], v16, s[28:31], s72 offen offset:0 nt\n\t"
    "buffer_load_dwordx4 v[92:95], v16, s[28:31], s72 offen offset:3584 nt\n\t"
    "s_branch LPVR0_%=\n\t"
    "LPVX_%=:\n\t"
    "v_and_b32_dpp v32, v57, v3 row_newbcast:4 row_mask:0xf bank_mask:0xf\n\t"
    "v_and_b32_dpp v33, v57, v4 row_newbcast:4 row_mask:0xf bank_mask:0xf\n\t"
    "v_and_b32_dpp v34, v57, v3 row_newbcast:5 row_mask:0xf bank_mask:0xf\n\t"
    "v_and_b32_dpp v35, v57, v4 row_newbcast:5 row_mask:0xf bank_mask:0xf\n\t"
    "v_and_b32_dpp v36, v57, v3 row_newbcast:6 row_mask:0xf bank_mask:0xf\n\t"
    "v_and_b32_dpp v37, v57, v4 row_newbcast:6 row_mask:0xf bank_mask:0xf\n\t"
    "v_smfmac_f32_16x16x64_bf16 v[48:51], v[32:35], v[160:167], v5\n\t"
    "v_and_b32_dpp v38, v57, v3 row_newbcast:7 row_mask:0xf bank_mask:0xf\n\t"
    "v_and_b32_dpp v39, v57, v4 row_newbcast:7 row_mask:0xf bank_mask:0xf\n\t"
    "v_smfmac_f32_16x16x64_bf16 v[52:55], v[32:35], v[168:175], v5\n\t"
    "s_nop 0\n\t"
    "v_smfmac_f32_16x16x64_bf16 v[48:51], v[36:39], v[176:183], v5\n\t"
    "v_smfmac_f32_16x16x64_bf16 v[52:55], v[36:39], v[184:191], v5\n\t"
    "s_add_u32 s96, s92, 0x10000\n\t"
    "s_add_u32 s97, s92, 0x18000\n\t"
    "s_add_u32 s98, s93, 0x10000\n\t"
    "s_add_u32 s99, s93, 0x18000\n\t"
    "buffer_load_dwordx4 v[160:163], v1, s[12:15], s96 offen offset:0 nt\n\t"
    "buffer_load_dwordx4 v[164:167], v1, s[12:15], s97 offen offset:0 nt\n\t"
    "buffer_load_dwordx4 v[168:171], v1, s[12:15], s96 offen offset:256 nt\n\t"
    "buffer_load_dwordx4 v[172:175], v1, s[12:15], s97 offen offset:256 nt\n\t"
    "buffer_load_dwordx4 v[176:179], v1, s[12:15], s98 offen offset:0 nt\n\t"
    "buffer_load_dwordx4 v[180:183], v1, s[12:15], s99 offen offset:0 nt\n\t"
    "buffer_load_dwordx4 v[184:187], v1, s[12:15], s98 offen offset:256 nt\n\t"
    "buffer_load_dwordx4 v[188:191], v1, s[12:15], s99 offen offset:256 nt\n\t"
    "s_waitcnt vmcnt(16)\n\t"
    "v_and_b32_dpp v32, v57, v3 row_newbcast:8 row_mask:0xf bank_mask:0xf\n\t"
    "v_and_b32_dpp v33, v57, v4 row_newbcast:8 row_mask:0xf bank_mask:0xf\n\t"
    "v_and_b32_dpp v34, v57, v3 row_newbcast:9 row_mask:0xf bank_mask:0xf\n\t"
    "v_and_b32_dpp v35, v57, v4 row_newbcast:9 row_mask:0xf bank_mask:0xf\n\t"
    "v_and_b32_dpp v36, v57, v3 row_newbcast:10 row_mask:0xf bank_mask:0xf\n\t"
    "v_and_b32_dpp v37, v57, v4 row_newbcast:10 row_mask:0xf bank_mask:0xf\n\t"
    "v_smfmac_f32_16x16x64_bf16 v[48:51], v[32:35], v[192:199], v5\n\t"
    "v_and_b32_dpp v38, v57, v3 row_newbcast:11 row_mask:0xf bank_mask:0xf\n\t"
    "v_and_b32_dpp v39, v57, v4 row_newbcast:11 row_mask:0xf bank_mask:0xf\n\t"
    "v_smfmac_f32_16x16x64_bf16 v[52:55], v[32:35], v[200:207], v5\n\t"
    "s_nop 0\n\t"
    "v_smfmac_f32_16x16x64_bf16 v[48:51], v[36:39], v[208:215], v5\n\t"
    "v_smfmac_f32_16x16x64_bf16 v[52:55], v[36:39], v[216:223], v5\n\t"
    "s_add_u32 s96, s94, 0x10000\n\t"
    "s_add_u32 s97, s94, 0x18000\n\t"
    "s_add_u32 s98, s95, 0x10000\n\t"
    "s_add_u32 s99, s95, 0x18000\n\t"
    "buffer_load_dwordx4 v[192:195], v1, s[12:15], s96 offen offset:0 nt\n\t"
    "buffer_load_dwordx4 v[196:199], v1, s[12:15], s97 offen offset:0 nt\n\t"
    "buffer_load_dwordx4 v[200:203], v1, s[12:15], s96 offen offset:256 nt\n\t"
    "buffer_load_dwordx4 v[204:207], v1, s[12:15], s97 offen offset:256 nt\n\t"
    "buffer_load_dwordx4 v[208:211], v1, s[12:15], s98 offen offset:0 nt\n\t"
    "buffer_load_dwordx4 v[212:215], v1, s[12:15], s99 offen offset:0 nt\n\t"
    "buffer_load_dwordx4 v[216:219], v1, s[12:15], s98 offen offset:256 nt\n\t"
    "buffer_load_dwordx4 v[220:223], v1, s[12:15], s99 offen offset:256 nt\n\t"
    "s_waitcnt vmcnt(16)\n\t"
    "v_and_b32_dpp v32, v57, v3 row_newbcast:12 row_mask:0xf bank_mask:0xf\n\t"
    "v_and_b32_dpp v33, v57, v4 row_newbcast:12 row_mask:0xf bank_mask:0xf\n\t"
    "v_and_b32_dpp v34, v57, v3 row_newbcast:13 row_mask:0xf bank_mask:0xf\n\t"
    "v_and_b32_dpp v35, v57, v4 row_newbcast:13 row_mask:0xf bank_mask:0xf\n\t"
    "v_and_b32_dpp v36, v57, v3 row_newbcast:14 row_mask:0xf bank_mask:0xf\n\t"
    "v_and_b32_dpp v37, v57, v4 row_newbcast:14 row_mask:0xf bank_mask:0xf\n\t"
    "v_smfmac_f32_16x16x64_bf16 v[48:51], v[32:35], v[224:231], v5\n\t"
    "v_and_b32_dpp v38, v57, v3 row_newbcast:15 row_mask:0xf bank_mask:0xf\n\t"
    "v_and_b32_dpp v39, v57, v4 row_newbcast:15 row_mask:0xf bank_mask:0xf\n\t"
    "v_smfmac_f32_16x16x64_bf16 v[52:55], v[32:35], v[232:239], v5\n\t"
    "s_nop 0\n\t"
    "v_smfmac_f32_16x16x64_bf16 v[48:51], v[36:39], v[240:247], v5\n\t"
    "v_smfmac_f32_16x16x64_bf16 v[52:55], v[36:39], v[248:255], v5\n\t"
    "s_add_u32 s96, s92, 0x20000\n\t"
    "s_add_u32 s97, s92, 0x28000\n\t"
    "s_add_u32 s98, s93, 0x20000\n\t"
    "s_add_u32 s99, s93, 0x28000\n\t"
    "buffer_load_dwordx4 v[224:227], v1, s[12:15], s96 offen offset:0 nt\n\t"
    "buffer_load_dwordx4 v[228:231], v1, s[12:15], s97 offen offset:0 nt\n\t"
    "buffer_load_dwordx4 v[232:235], v1, s[12:15], s96 offen offset:256 nt\n\t"
    "buffer_load_dwordx4 v[236:239], v1, s[12:15], s97 offen offset:256 nt\n\t"
    "buffer_load_dwordx4 v[240:243], v1, s[12:15], s98 offen offset:0 nt\n\t"
    "buffer_load_dwordx4 v[244:247], v1, s[12:15], s99 offen offset:0 nt\n\t"
    "buffer_load_dwordx4 v[248:251], v1, s[12:15], s98 offen offset:256 nt\n\t"
    "buffer_load_dwordx4 v[252:255], v1, s[12:15], s99 offen offset:256 nt\n\t"
    "s_branch LPVR0_%=\n\t"
    "LHK2_%=:\n\t"
    "s_waitcnt vmcnt(17)\n\t"
    "s_branch LHR2_%=\n\t"
    "LHK3_%=:\n\t"
    "s_waitcnt vmcnt(9)\n\t"
    "s_branch LHR3_%=\n\t"
    "LHK4_%=:\n\t"
    "s_cmp_eq_u32 s51, 0\n\t"
    "s_cbranch_scc1 LHK4A_%=\n\t"
    "s_call_b64 s[66:67], LBB_%=\n\t"
    "s_waitcnt vmcnt(5)\n\t"
    "s_branch LHK4B_%=\n\t"
    "LHK4A_%=:\n\t"
    "s_waitcnt vmcnt(1)\n\t"
    "LHK4B_%=:\n\t"
    "s_bitcmp1_b32 s60, 3\n\t"
    "s_branch LHR4_%=\n\t"
    "LATTL_%=:\n\t"
    "ds_read_b128 v[18:21], v2 offset:0\n\t"
    "ds_read_b128 v[22:25], v2 offset:1024\n\t"
    "ds_read_b128 v[26:29], v2 offset:2048\n\t"
    "v_mov_b32 v60, 0xff800000\n\t"
    "v_mov_b32 v61, 0xff800000\n\t"
    "v_mov_b32 v62, 0xff800000\n\t"
    "v_mov_b32 v63, 0xff800000\n\t"
    "s_waitcnt lgkmcnt(2)\n\t"
    "v_mfma_f32_16x16x32_bf16 v[48:51], v[32:35], v[18:21], 0\n\t"
    "ds_read_b128 v[18:21], v2 offset:3072\n\t"
    "s_waitcnt lgkmcnt(2)\n\t"
    "v_mfma_f32_16x16x32_bf16 v[48:51], v[36:39], v[22:25], v[48:51]\n\t"
    "ds_read_b128 v[22:25], v2 offset:4096\n\t"
    "s_waitcnt lgkmcnt(2)\n\t"
    "v_mfma_f32_16x16x32_bf16 v[48:51], v[40:43], v[26:29], v[48:51]\n\t"
    "ds_read_b128 v[26:29], v2 offset:5120\n\t"
    "s_waitcnt lgkmcnt(2)\n\t"
    "v_mfma_f32_16x16x32_bf16 v[48:51], v[44:47], v[18:21], v[48:51]\n\t"
    "ds_read_b128 v[18:21], v2 offset:6144\n\t"
    "s_waitcnt lgkmcnt(2)\n\t"
    "v_mfma_f32_16x16x32_bf16 v[52:55], v[32:35], v[22:25], 0\n\t"
    "ds_read_b128 v[22:25], v2 offset:7168\n\t"
    "s_waitcnt lgkmcnt(2)\n\t"
    "v_mfma_f32_16x16x32_bf16 v[52:55], v[36:39], v[26:29], v[52:55]\n\t"
    "ds_read_b128 v[26:29], v2 offset:8192\n\t"
    "s_waitcnt lgkmcnt(2)\n\t"
    "v_mfma_f32_16x16x32_bf16 v[52:55], v[40:43], v[18:21], v[52:55]\n\t"
    "ds_read_b128 v[18:21], v2 offset:9216\n\t"
    "s_waitcnt lgkmcnt(2)\n\t"
    "v_mfma_f32_16x16x32_bf16 v[52:55], v[44:47], v[22:25], v[52:55]\n\t"
    "ds_read_b128 v[22:25], v2 offset:10240\n\t"
    "s_waitcnt lgkmcnt(2)\n\t"
    "v_mfma_f32_16x16x32_bf16 v[56:59], v[32:35], v[26:29], 0\n\t"
    "ds_read_b128 v[26:29], v2 offset:11264\n\t"
    "s_waitcnt lgkmcnt(2)\n\t"
    "v_mfma_f32_16x16x32_bf16 v[56:59], v[36:39], v[18:21], v[56:59]\n\t"
    "ds_read_b128 v[18:21], v2 offset:12288\n\t"
    "s_waitcnt lgkmcnt(2)\n\t"
    "v_mfma_f32_16x16x32_bf16 v[56:59], v[40:43], v[22:25], v[56:59]\n\t"
    "ds_read_b128 v[22:25], v2 offset:13312\n\t"
    "s_waitcnt lgkmcnt(2)\n\t"
    "v_mfma_f32_16x16x32_bf16 v[56:59], v[44:47], v[26:29], v[56:59]\n\t"
    "ds_read_b128 v[26:29], v2 offset:14336\n\t"
    "v_add_f32_dpp v60, v50, v48 row_ror:8 row_mask:0x1 bank_mask:0x3\n\t"
    "v_add_f32_dpp v61, v51, v49 row_ror:8 row_mask:0x1 bank_mask:0x3\n\t"
    "s_waitcnt lgkmcnt(2)\n\t"
    "v_mfma_f32_16x16x32_bf16 v[48:51], v[32:35], v[18:21], 0\n\t"
    "ds_read_b128 v[18:21], v2 offset:15360\n\t"
    "s_waitcnt lgkmcnt(2)\n\t"
    "v_mfma_f32_16x16x32_bf16 v[48:51], v[36:39], v[22:25], v[48:51]\n\t"
    "s_waitcnt lgkmcnt(1)\n\t"
    "v_mfma_f32_16x16x32_bf16 v[48:51], v[40:43], v[26:29], v[48:51]\n\t"
    "s_waitcnt lgkmcnt(0)\n\t"
    "v_mfma_f32_16x16x32_bf16 v[48:51], v[44:47], v[18:21], v[48:51]\n\t"
    "ds_read_b128 v[18:21], v2 offset:16384\n\t"
    "ds_read_b128 v[22:25], v2 offset:17408\n\t"
    "ds_read_b128 v[40:43], v2 offset:18432\n\t"
    "ds_read_b128 v[44:47], v2 offset:19456\n\t"
    "v_add_f32_dpp v60, v54, v52 row_ror:8 row_mask:0x2 bank_mask:0x3\n\t"
    "v_add_f32_dpp v61, v55, v53 row_ror:8 row_mask:0x2 bank_mask:0x3\n\t"
    "v_add_f32_dpp v60, v58, v56 row_ror:8 row_mask:0x4 bank_mask:0x3\n\t"
    "v_add_f32_dpp v61, v59, v57 row_ror:8 row_mask:0x4 bank_mask:0x3\n\t"
    "v_add_f32_dpp v60, v50, v48 row_ror:8 row_mask:0x8 bank_mask:0x3\n\t"
    "v_add_f32_dpp v61, v51, v49 row_ror:8 row_mask:0x8 bank_mask:0x3\n\t"
    "s_branch LSOFT_%=\n\t"
    "LPVLDS_%=:\n\t"
    "v_and_b32_dpp v32, v56, v3 row_newbcast:0 row_mask:0xf bank_mask:0xf\n\t"
    "v_and_b32_dpp v33, v56, v4 row_newbcast:0 row_mask:0xf bank_mask:0xf\n\t"
    "v_permlane32_swap_b32 v31, v58\n\t"
    "v_add_f32 v31, v31, v58\n\t"
    "v_mov_b32 v58, v31\n\t"
    "v_and_b32_dpp v34, v56, v3 row_newbcast:1 row_mask:0xf bank_mask:0xf\n\t"
    "v_and_b32_dpp v35, v56, v4 row_newbcast:1 row_mask:0xf bank_mask:0xf\n\t"
    "v_permlane16_swap_b32 v58, v31\n\t"
    "v_add_f32 v31, v31, v58\n\t"
    "v_and_b32_dpp v36, v56, v3 row_newbcast:2 row_mask:0xf bank_mask:0xf\n\t"
    "v_and_b32_dpp v37, v56, v4 row_newbcast:2 row_mask:0xf bank_mask:0xf\n\t"
    "v_add_f32_dpp v31, v31, v31 row_ror:8 row_mask:0xf bank_mask:0xf\n\t"
    "s_waitcnt lgkmcnt(2)\n\t"
    "v_smfmac_f32_16x16x64_bf16 v[48:51], v[32:35], v[18:25], v5\n\t"
    "ds_read_b128 v[18:21], v2 offset:20480\n\t"
    "ds_read_b128 v[22:25], v2 offset:21504\n\t"
    "v_and_b32_dpp v38, v56, v3 row_newbcast:3 row_mask:0xf bank_mask:0xf\n\t"
    "v_add_f32_dpp v31, v31, v31 row_ror:4 row_mask:0xf bank_mask:0xf\n\t"
    "v_and_b32_dpp v39, v56, v4 row_newbcast:3 row_mask:0xf bank_mask:0xf\n\t"
    "s_waitcnt lgkmcnt(2)\n\t"
    "v_smfmac_f32_16x16x64_bf16 v[52:55], v[32:35], v[40:47], v5\n\t"
    "ds_read_b128 v[40:43], v2 offset:22528\n\t"
    "ds_read_b128 v[44:47], v2 offset:23552\n\t"
    "v_add_f32_dpp v31, v31, v31 row_ror:2 row_mask:0xf bank_mask:0xf\n\t"
    "s_waitcnt lgkmcnt(2)\n\t"
    "v_smfmac_f32_16x16x64_bf16 v[48:51], v[36:39], v[18:25], v5\n\t"
    "ds_read_b128 v[18:21], v2 offset:24576\n\t"
    "ds_read_b128 v[22:25], v2 offset:25600\n\t"
    "v_and_b32_dpp v32, v56, v3 row_newbcast:4 row_mask:0xf bank_mask:0xf\n\t"
    "v_add_f32_dpp v31, v31, v31 row_ror:1 row_mask:0xf bank_mask:0xf\n\t"
    "s_waitcnt lgkmcnt(2)\n\t"
    "v_smfmac_f32_16x16x64_bf16 v[52:55], v[36:39], v[40:47], v5\n\t"
    "ds_read_b128 v[40:43], v2 offset:26624\n\t"
    "ds_read_b128 v[44:47], v2 offset:27648\n\t"
    "ds_write_b64 v12, v[30:31]\n\t"
    "v_and_b32_dpp v33, v56, v4 row_newbcast:4 row_mask:0xf bank_mask:0xf\n\t"
    "v_and_b32_dpp v34, v56, v3 row_newbcast:5 row_mask:0xf bank_mask:0xf\n\t"
    "v_and_b32_dpp v35, v56, v4 row_newbcast:5 row_mask:0xf bank_mask:0xf\n\t"
    "v_and_b32_dpp v36, v56, v3 row_newbcast:6 row_mask:0xf bank_mask:0xf\n\t"
    "v_and_b32_dpp v37, v56, v4 row_newbcast:6 row_mask:0xf bank_mask:0xf\n\t"
    "s_waitcnt lgkmcnt(3)\n\t"
    "v_smfmac_f32_16x16x64_bf16 v[48:51], v[32:35], v[18:25], v5\n\t"
    "ds_read_b128 v[18:21], v2 offset:28672\n\t"
    "ds_read_b128 v[22:25], v2 offset:29696\n\t"
    "v_and_b32_dpp v38, v56, v3 row_newbcast:7 row_mask:0xf bank_mask:0xf\n\t"
    "v_and_b32_dpp v39, v56, v4 row_newbcast:7 row_mask:0xf bank_mask:0xf\n\t"
    "s_waitcnt lgkmcnt(2)\n\t"
    "s_cbranch_execz LNB13_%=\n\t"
    "s_barrier\n\t"
    "LNB13_%=:\n\t"
    "ds_read_b64 v[60:61], v11\n\t"
    "s_waitcnt lgkmcnt(4)\n\t"
    "v_smfmac_f32_16x16x64_bf16 v[52:55], v[32:35], v[40:47], v5\n\t"
    "ds_read_b128 v[40:43], v2 offset:30720\n\t"
    "ds_read_b128 v[44:47], v2 offset:31744\n\t"
    "s_waitcnt lgkmcnt(3)\n\t"
    "v_smfmac_f32_16x16x64_bf16 v[48:51], v[36:39], v[18:25], v5\n\t"
    "s_waitcnt lgkmcnt(0)\n\t"
    "v_smfmac_f32_16x16x64_bf16 v[52:55], v[36:39], v[40:47], v5\n\t"
    "s_waitcnt lgkmcnt(2)\n\t"
    "v_max_f32_dpp v62, v60, v60 quad_perm:[1,0,3,2] row_mask:0xf bank_mask:0xf\n\t"
    "s_nop 1\n\t"
    "v_max_f32_dpp v62, v62, v62 quad_perm:[2,3,0,1] row_mask:0xf bank_mask:0xf\n\t"
    "v_sub_f32 v60, v60, v62\n\t"
    "v_sub_f32 v59, v30, v62\n\t"
    "v_cvt_i32_f32 v60, v60\n\t"
    "v_cvt_i32_f32 v59, v59\n\t"
    "v_ldexp_f32 v61, v61, v60\n\t"
    "v_min_u32 v62, -2, v62\n\t"
    "s_nop 0\n\t"
    "v_add_f32_dpp v63, v61, v61 quad_perm:[1,0,3,2] row_mask:0xf bank_mask:0xf\n\t"
    "s_nop 1\n\t"
    "v_add_f32_dpp v63, v63, v63 quad_perm:[2,3,0,1] row_mask:0xf bank_mask:0xf\n\t"
    "v_min_u32 v63, -2, v63\n\t"
    "s_cbranch_execz LNB14_%=\n\t"
    "buffer_store_dwordx2 v[62:63], v10, s[24:27], s56 offen sc1\n\t"
    "LNB14_%=:\n\t"
    "s_branch LMERGE_%=\n\t"
    "LBB_%=:\n\t"
    "s_add_u32 s79, s59, s90\n\t"
    "s_and_b32 s79, s79, 3\n\t"
    "s_cmp_ge_u32 s59, 4\n\t"
    "s_cbranch_scc1 LBBV_%=\n\t"
    "s_lshl_b32 s78, s79, 12\n\t"
    "s_add_u32 m0, s78, s83\n\t"
    "s_lshr_b32 s79, s79, 1\n\t"
    "s_mul_i32 s79, s79, 0x6000\n\t"
    "s_add_u32 s78, s78, s79\n\t"
    "s_add_u32 s78, s78, s49\n\t"
    "buffer_load_dwordx4 v0, s[16:19], s78 offen nt lds\n\t"
    "s_add_u32 m0, m0, 0x400\n\t"
    "s_add_u32 s78, s78, 0x80\n\t"
    "buffer_load_dwordx4 v0, s[16:19], s78 offen nt lds\n\t"
    "s_add_u32 m0, m0, 0x400\n\t"
    "s_add_u32 s78, s78, 0x80\n\t"
    "buffer_load_dwordx4 v0, s[16:19], s78 offen nt lds\n\t"
    "s_add_u32 m0, m0, 0x400\n\t"
    "s_add_u32 s78, s78, 0x80\n\t"
    "buffer_load_dwordx4 v0, s[16:19], s78 offen nt lds\n\t"
    "s_add_u32 s59, s59, 1\n\t"
    "s_setpc_b64 s[66:67]\n\t"
    "LBBV_%=:\n\t"
    "s_lshl_b32 s78, s59, 12\n\t"
    "s_add_u32 m0, s78, s83\n\t"
    "s_lshl_b32 s78, s79, 10\n\t"
    "s_add_u32 s78, s78, s49\n\t"
    "s_add_u32 s79, s78, 0x8000\n\t"
    "buffer_load_dwordx4 v1, s[20:23], s78 offen nt lds\n\t"
    "s_add_u32 m0, m0, 0x400\n\t"
    "s_add_u32 s78, s78, 0x100\n\t"
    "buffer_load_dwordx4 v1, s[20:23], s79 offen nt lds\n\t"
    "s_add_u32 m0, m0, 0x400\n\t"
    "s_add_u32 s79, s79, 0x100\n\t"
    "buffer_load_dwordx4 v1, s[20:23], s78 offen nt lds\n\t"
    "s_add_u32 m0, m0, 0x400\n\t"
    "s_add_u32 s59, s59, 1\n\t"
    "buffer_load_dwordx4 v1, s[20:23], s79 offen nt lds\n\t"
    "s_setpc_b64 s[66:67]\n\t"
    "LWLY_%=:\n\t"
    "buffer_load_dwordx4 v[64:67], v16, s[68:71], s52 offen offset:0 nt\n\t"
    "buffer_load_dwordx4 v[68:71], v16, s[68:71], s52 offen offset:3584 nt\n\t"
    "s_add_u32 s72, s52, 0x3800\n\t"
    "buffer_load_dwordx4 v[72:75], v16, s[68:71], s72 offen offset:0 nt\n\t"
    "buffer_load_dwordx4 v[76:79], v16, s[68:71], s72 offen offset:3584 nt\n\t"
    "s_add_u32 s72, s52, 0x7000\n\t"
    "buffer_load_dwordx4 v[80:83], v16, s[68:71], s72 offen offset:0 nt\n\t"
    "buffer_load_dwordx4 v[84:87], v16, s[68:71], s72 offen offset:3584 nt\n\t"
    "s_add_u32 s72, s52, 0xa800\n\t"
    "buffer_load_dwordx4 v[88:91], v16, s[68:71], s72 offen offset:0 nt\n\t"
    "buffer_load_dwordx4 v[92:95], v16, s[68:71], s72 offen offset:3584 nt\n\t"
    "s_setpc_b64 s[66:67]\n\t"
    "LWLQ_%=:\n\t"
    "s_add_u32 s72, s52, 0x0\n\t"
    "buffer_load_dwordx2 v[64:65], v16, s[68:71], s72 offen nt\n\t"
    "s_add_u32 s72, s52, 0x4000\n\t"
    "buffer_load_dwordx2 v[66:67], v16, s[68:71], s72 offen nt\n\t"
    "s_add_u32 s72, s52, 0x10000\n\t"
    "buffer_load_dwordx2 v[72:73], v16, s[68:71], s72 offen nt\n\t"
    "s_add_u32 s72, s52, 0x14000\n\t"
    "buffer_load_dwordx2 v[74:75], v16, s[68:71], s72 offen nt\n\t"
    "s_add_u32 s72, s52, 0x20000\n\t"
    "buffer_load_dwordx2 v[80:81], v16, s[68:71], s72 offen nt\n\t"
    "s_add_u32 s72, s52, 0x24000\n\t"
    "buffer_load_dwordx2 v[82:83], v16, s[68:71], s72 offen nt\n\t"
    "s_add_u32 s72, s52, 0x30000\n\t"
    "buffer_load_dwordx2 v[88:89], v16, s[68:71], s72 offen nt\n\t"
    "s_add_u32 s72, s52, 0x34000\n\t"
    "buffer_load_dwordx2 v[90:91], v16, s[68:71], s72 offen nt\n\t"
    "s_add_u32 s72, s52, 0x80000\n\t"
    "buffer_load_dwordx2 v[68:69], v16, s[68:71], s72 offen nt\n\t"
    "s_add_u32 s72, s52, 0x84000\n\t"
    "buffer_load_dwordx2 v[70:71], v16, s[68:71], s72 offen nt\n\t"
    "s_add_u32 s72, s52, 0x90000\n\t"
    "buffer_load_dwordx2 v[76:77], v16, s[68:71], s72 offen nt\n\t"
    "s_add_u32 s72, s52, 0x94000\n\t"
    "buffer_load_dwordx2 v[78:79], v16, s[68:71], s72 offen nt\n\t"
    "s_add_u32 s72, s52, 0xa0000\n\t"
    "buffer_load_dwordx2 v[84:85], v16, s[68:71], s72 offen nt\n\t"
    "s_add_u32 s72, s52, 0xa4000\n\t"
    "buffer_load_dwordx2 v[86:87], v16, s[68:71], s72 offen nt\n\t"
    "s_add_u32 s72, s52, 0xb0000\n\t"
    "buffer_load_dwordx2 v[92:93], v16, s[68:71], s72 offen nt\n\t"
    "s_add_u32 s72, s52, 0xb4000\n\t"
    "buffer_load_dwordx2 v[94:95], v16, s[68:71], s72 offen nt\n\t"
    "s_setpc_b64 s[66:67]\n\t"
    "LCH_%=:\n\t"
    "s_cmp_eq_u32 s51, 1\n\t"
    "s_cbranch_scc1 LCQ_%=\n\t"
    "s_setpc_b64 s[64:65]\n\t"
    "LCY_%=:\n\t"
    "s_cbranch_execz LYT_%=\n\t"
    "buffer_load_dwordx2 v[48:49], v20, s[24:27], s72 offen offset:0 sc1\n\t"
    "buffer_load_dwordx2 v[50:51], v20, s[24:27], s72 offen offset:256 sc1\n\t"
    "buffer_load_dwordx2 v[52:53], v20, s[24:27], s72 offen offset:2048 sc1\n\t"
    "buffer_load_dwordx2 v[54:55], v20, s[24:27], s72 offen offset:2304 sc1\n\t"
    "buffer_store_dword v31, v9, s[24:27], s85 offen sc1\n\t"
    "s_add_u32 s73, s72, 0x4000\n\t"
    "s_mov_b32 s76, 0x800080\n\t"
    "s_mov_b64 s[74:75], -1\n\t"
    "s_waitcnt vmcnt(1)\n\t"
    "v_max3_u32 v30, v48, v49, v50\n\t"
    "v_max3_u32 v30, v51, v52, v30\n\t"
    "v_max3_u32 v30, v53, v54, v30\n\t"
    "v_max_u32 v30, v55, v30\n\t"
    "v_cmp_eq_u32 vcc, -1, v30\n\t"
    "s_cbranch_vccz LY1E_%=\n\t"
    "LY1_%=:\n\t"
    "buffer_load_dwordx2 v[48:49], v20, s[24:27], s72 offen offset:0 sc1\n\t"
    "buffer_load_dwordx2 v[50:51], v20, s[24:27], s72 offen offset:256 sc1\n\t"
    "buffer_load_dwordx2 v[52:53], v20, s[24:27], s72 offen offset:2048 sc1\n\t"
    "buffer_load_dwordx2 v[54:55], v20, s[24:27], s72 offen offset:2304 sc1\n\t"
    "buffer_load_dwordx4 v[32:35], v18, s[24:27], s72 offen sc1\n\t"
    "buffer_load_dwordx4 v[36:39], v19, s[24:27], s72 offen sc1\n\t"
    "buffer_load_dwordx4 v[40:43], v18, s[24:27], s73 offen sc1\n\t"
    "buffer_load_dwordx4 v[44:47], v19, s[24:27], s73 offen sc1\n\t"
    "s_waitcnt vmcnt(0)\n\t"
    "LY1C_%=:\n\t"
    "v_max3_u32 v30, v48, v49, v50\n\t"
    "v_max3_u32 v30, v51, v52, v30\n\t"
    "v_max3_u32 v30, v53, v54, v30\n\t"
    "v_max_u32 v30, v55, v30\n\t"
    "v_cmp_eq_u32 vcc, -1, v30\n\t"
    "s_cbranch_vccz LY1D_%=\n\t"
    "s_sub_u32 s63, s63, 1\n\t"
    "s_cbranch_scc0 LY1_%=\n\t"
    "s_mov_b32 s63, 0\n\t"
    "s_mov_b64 vcc, 0\n\t"
    "LY1D_%=:\n\t"
    "v_max3_u32 v30, v32, v33, v34\n\t"
    "v_max3_u32 v30, v35, v36, v30\n\t"
    "v_max3_u32 v30, v37, v38, v30\n\t"
    "v_max3_u32 v30, v39, v40, v30\n\t"
    "v_max3_u32 v30, v41, v42, v30\n\t"
    "v_max3_u32 v30, v43, v44, v30\n\t"
    "v_max3_u32 v30, v45, v46, v30\n\t"
    "v_max_u32 v30, v47, v30\n\t"
    "v_cmp_eq_u32 vcc, -1, v30\n\t"
    "s_mov_b64 s[74:75], vcc\n\t"
    "s_cbranch_vccz LYT_%=\n\t"
    "LY1E_%=:\n\t"
    "buffer_load_dwordx4 v[32:35], v18, s[24:27], s72 offen sc1\n\t"
    "buffer_load_dwordx4 v[36:39], v19, s[24:27], s72 offen sc1\n\t"
    "buffer_load_dwordx4 v[40:43], v18, s[24:27], s73 offen sc1\n\t"
    "buffer_load_dwordx4 v[44:47], v19, s[24:27], s73 offen sc1\n\t"
    "LYT_%=:\n\t"
    "v_max3_f32 v30, v48, v50, v52\n\t"
    "v_max_f32 v30, v30, v54\n\t"
    "s_nop 1\n\t"
    "v_max_f32_dpp v30, v30, v30 row_ror:8 row_mask:0xf bank_mask:0xf\n\t"
    "v_mov_b32 v31, v30\n\t"
    "s_nop 1\n\t"
    "v_permlane16_swap_b32 v31, v30\n\t"
    "v_max_f32 v30, v30, v31\n\t"
    "v_mov_b32 v31, v30\n\t"
    "s_nop 1\n\t"
    "v_permlane32_swap_b32 v31, v30\n\t"
    "v_max_f32 v30, v30, v31\n\t"
    "v_sub_f32 v48, v48, v30\n\t"
    "v_sub_f32 v50, v50, v30\n\t"
    "v_sub_f32 v52, v52, v30\n\t"
    "v_sub_f32 v54, v54, v30\n\t"
    "v_cvt_i32_f32 v48, v48\n\t"
    "v_cvt_i32_f32 v50, v50\n\t"
    "v_cvt_i32_f32 v52, v52\n\t"
    "v_cvt_i32_f32 v54, v54\n\t"
    "v_ldexp_f32 v49, v49, v48\n\t"
    "v_ldexp_f32 v51, v51, v50\n\t"
    "v_ldexp_f32 v53, v53, v52\n\t"
    "v_ldexp_f32 v55, v55, v54\n\t"
    "v_add_u32 v48, 0x7f, v48\n\t"
    "v_add_u32 v50, 0x7f, v50\n\t"
    "v_add_u32 v52, 0x7f, v52\n\t"
    "v_add_u32 v54, 0x7f, v54\n\t"
    "v_max_i32 v48, 0, v48\n\t"
    "v_max_i32 v50, 0, v50\n\t"
    "v_max_i32 v52, 0, v52\n\t"
    "v_max_i32 v54, 0, v54\n\t"
    "v_mul_u32_u24 v48, s76, v48\n\t"
    "v_mul_u32_u24 v50, s76, v50\n\t"
    "v_mul_u32_u24 v52, s76, v52\n\t"
    "v_mul_u32_u24 v54, s76, v54\n\t"
    "v_add_f32 v31, v49, v51\n\t"
    "v_add_f32 v53, v53, v55\n\t"
    "v_and_b32 v56, v48, v3\n\t"
    "v_and_b32 v57, v48, v4\n\t"
    "v_and_b32 v58, v50, v3\n\t"
    "v_and_b32 v59, v50, v4\n\t"
    "v_and_b32 v60, v52, v3\n\t"
    "v_and_b32 v61, v52, v4\n\t"
    "v_and_b32 v62, v54, v3\n\t"
    "v_and_b32 v63, v54, v4\n\t"
    "v_add_f32 v31, v31, v53\n\t"
    "v_mov_b32 v48, 0\n\t"
    "v_mov_b32 v49, 0\n\t"
    "v_add_f32_dpp v31, v31, v31 row_ror:8 row_mask:0xf bank_mask:0xf\n\t"
    "v_mov_b32 v30, v31\n\t"
    "v_mov_b32 v50, 0\n\t"
    "v_mov_b32 v51, 0\n\t"
    "v_permlane16_swap_b32 v30, v31\n\t"
    "v_add_f32 v31, v31, v30\n\t"
    "v_mov_b32 v30, v31\n\t"
    "v_mov_b32 v26, 0\n\t"
    "v_mov_b32 v27, 0\n\t"
    "v_permlane32_swap_b32 v30, v31\n\t"
    "v_add_f32 v31, v31, v30\n\t"
    "v_rcp_f32 v31, v31\n\t"
    "v_mov_b32 v28, 0\n\t"
    "v_mov_b32 v29, 0\n\t"
    "s_cbranch_execz LYG_%=\n\t"
    "s_cmp_eq_u64 s[74:75], 0\n\t"
    "s_cbranch_scc1 LYG_%=\n\t"
    "s_waitcnt vmcnt(0)\n\t"
    "s_branch LY2C_%=\n\t"
    "LY2_%=:\n\t"
    "buffer_load_dwordx4 v[32:35], v18, s[24:27], s72 offen sc1\n\t"
    "buffer_load_dwordx4 v[36:39], v19, s[24:27], s72 offen sc1\n\t"
    "buffer_load_dwordx4 v[40:43], v18, s[24:27], s73 offen sc1\n\t"
    "buffer_load_dwordx4 v[44:47], v19, s[24:27], s73 offen sc1\n\t"
    "s_waitcnt vmcnt(0)\n\t"
    "LY2C_%=:\n\t"
    "v_max3_u32 v30, v32, v33, v34\n\t"
    "v_max3_u32 v30, v35, v36, v30\n\t"
    "v_max3_u32 v30, v37, v38, v30\n\t"
    "v_max3_u32 v30, v39, v40, v30\n\t"
    "v_max3_u32 v30, v41, v42, v30\n\t"
    "v_max3_u32 v30, v43, v44, v30\n\t"
    "v_max3_u32 v30, v45, v46, v30\n\t"
    "v_max_u32 v30, v47, v30\n\t"
    "v_cmp_eq_u32 vcc, -1, v30\n\t"
    "s_cbranch_vccz LYG_%=\n\t"
    "s_sub_u32 s63, s63, 1\n\t"
    "s_cbranch_scc0 LY2_%=\n\t"
    "s_mov_b32 s63, 0\n\t"
    "s_mov_b64 vcc, 0\n\t"
    "LYG_%=:\n\t"
    "v_smfmac_f32_16x16x64_bf16 v[48:51], v[56:59], v[32:39], v5\n\t"
    "v_smfmac_f32_16x16x64_bf16 v[48:51], v[60:63], v[40:47], v5\n\t"
    "s_cmp_eq_u32 s84, 0\n\t"
    "s_nop 6\n\t"
    "v_permlane32_swap_b32 v48, v50\n\t"
    "v_permlane32_swap_b32 v49, v51\n\t"
    "s_nop 0\n\t"
    "v_add_f32_dpp v52, v50, v48 row_ror:8 row_mask:0xf bank_mask:0xf\n\t"
    "v_add_f32_dpp v53, v51, v49 row_ror:8 row_mask:0xf bank_mask:0xf\n\t"
    "v_cvt_pk_bf16_f32 v52, v52, v53\n\t"
    "ds_write_b32 v21, v52\n\t"
    "ds_read_b128 v[32:35], v22\n\t"
    "ds_read_b128 v[36:39], v22 offset:16\n\t"
    "s_waitcnt lgkmcnt(1)\n\t"
    "v_and_b32 v40, v32, v3\n\t"
    "v_and_b32 v41, v32, v4\n\t"
    "v_and_b32 v42, v33, v3\n\t"
    "v_and_b32 v43, v33, v4\n\t"
    "v_and_b32 v44, v34, v3\n\t"
    "v_and_b32 v45, v34, v4\n\t"
    "v_smfmac_f32_16x16x64_bf16 v[26:29], v[40:43], v[64:71], v5\n\t"
    "v_and_b32 v46, v35, v3\n\t"
    "v_and_b32 v47, v35, v4\n\t"
    "s_waitcnt lgkmcnt(0)\n\t"
    "v_and_b32 v48, v36, v3\n\t"
    "v_smfmac_f32_16x16x64_bf16 v[26:29], v[44:47], v[72:79], v5\n\t"
    "v_and_b32 v49, v36, v4\n\t"
    "v_and_b32 v50, v37, v3\n\t"
    "v_and_b32 v51, v37, v4\n\t"
    "v_and_b32 v52, v38, v3\n\t"
    "v_and_b32 v53, v38, v4\n\t"
    "v_smfmac_f32_16x16x64_bf16 v[26:29], v[48:51], v[80:87], v5\n\t"
    "v_and_b32 v54, v39, v3\n\t"
    "v_and_b32 v55, v39, v4\n\t"
    "s_nop 1\n\t"
    "v_smfmac_f32_16x16x64_bf16 v[26:29], v[52:55], v[88:95], v5\n\t"
    "s_nop 7\n\t"
    "v_permlane32_swap_b32 v26, v28\n\t"
    "v_permlane32_swap_b32 v27, v29\n\t"
    "s_nop 0\n\t"
    "v_add_f32_dpp v32, v28, v26 row_ror:8 row_mask:0xf bank_mask:0xf\n\t"
    "v_add_f32_dpp v33, v29, v27 row_ror:8 row_mask:0xf bank_mask:0xf\n\t"
    "ds_write_b64 v23, v[32:33]\n\t"
    "s_waitcnt lgkmcnt(0)\n\t"
    "s_cbranch_execz LNB15_%=\n\t"
    "s_barrier\n\t"
    "LNB15_%=:\n\t"
    "s_cbranch_scc0 LCR_%=\n\t"
    "ds_read2_b64 v[32:35], v24 offset0:0 offset1:32\n\t"
    "ds_read2_b64 v[36:39], v24 offset0:64 offset1:96\n\t"
    "s_waitcnt lgkmcnt(1)\n\t"
    "v_pk_add_f32 v[32:33], v[32:33], v[34:35]\n\t"
    "s_waitcnt lgkmcnt(0)\n\t"
    "v_pk_add_f32 v[36:37], v[36:37], v[38:39]\n\t"
    "v_pk_add_f32 v[32:33], v[32:33], v[36:37]\n\t"
    "v_mul_f32 v32, v32, v31\n\t"
    "v_mul_f32 v33, v33, v31\n\t"
    "v_cvt_pk_bf16_f32 v32, v32, v33\n\t"
    "v_min_u32 v32, -2, v32\n\t"
    "s_cbranch_execz LCR_%=\n\t"
    "buffer_store_dword v32, v25, s[24:27], s57 offen sc1\n\t"
    "LCR_%=:\n\t"
    "s_setpc_b64 s[64:65]\n\t"
    "LCQ_%=:\n\t"
    "ds_read_b128 v[26:29], v17 offset:2048\n\t"
    "ds_read_b128 v[60:63], v17 offset:3072\n\t"
    "s_add_u32 s72, s57, s58\n\t"
    "s_add_u32 s73, s57, 0x10000\n\t"
    "s_mov_b32 s76, 0x3f803f80\n\t"
    "s_waitcnt lgkmcnt(0)\n\t"
    "v_mov_b32 v48, v26\n\t"
    "v_mov_b32 v52, 0\n\t"
    "v_mov_b32 v49, v27\n\t"
    "v_mov_b32 v53, 0\n\t"
    "v_mov_b32 v50, v28\n\t"
    "v_mov_b32 v54, 0\n\t"
    "v_mov_b32 v51, v29\n\t"
    "v_mov_b32 v55, 0\n\t"
    "v_and_b32 v56, s76, v3\n\t"
    "v_and_b32 v57, s76, v4\n\t"
    "v_and_b32 v58, s76, v3\n\t"
    "v_and_b32 v59, s76, v4\n\t"
    "v_permlane32_swap_b32 v48, v52\n\t"
    "v_mov_b32 v26, 0\n\t"
    "v_permlane32_swap_b32 v49, v53\n\t"
    "v_mov_b32 v27, 0\n\t"
    "v_permlane32_swap_b32 v50, v54\n\t"
    "v_mov_b32 v28, 0\n\t"
    "v_permlane32_swap_b32 v51, v55\n\t"
    "v_mov_b32 v29, 0\n\t"
    "s_cbranch_execz LQG_%=\n\t"
    "s_bitcmp1_b32 s60, 0\n\t"
    "s_cbranch_scc1 LQZ_%=\n\t"
    "LQ1_%=:\n\t"
    "buffer_load_dwordx4 v[32:35], v18, s[24:27], s72 offen sc1\n\t"
    "buffer_load_dwordx4 v[36:39], v18, s[24:27], s72 offen offset:1024 sc1\n\t"
    "buffer_load_dwordx4 v[40:43], v19, s[24:27], s72 offen sc1\n\t"
    "buffer_load_dwordx4 v[44:47], v19, s[24:27], s72 offen offset:1024 sc1\n\t"
    "s_waitcnt vmcnt(0)\n\t"
    "LQ1C_%=:\n\t"
    "v_max3_u32 v30, v32, v33, v34\n\t"
    "v_max3_u32 v30, v35, v36, v30\n\t"
    "v_max3_u32 v30, v37, v38, v30\n\t"
    "v_max3_u32 v30, v39, v40, v30\n\t"
    "v_max3_u32 v30, v41, v42, v30\n\t"
    "v_max3_u32 v30, v43, v44, v30\n\t"
    "v_max3_u32 v30, v45, v46, v30\n\t"
    "v_max_u32 v30, v47, v30\n\t"
    "v_cmp_eq_u32 vcc, -1, v30\n\t"
    "s_cbranch_vccz LQG_%=\n\t"
    "s_sub_u32 s63, s63, 1\n\t"
    "s_cbranch_scc0 LQ1_%=\n\t"
    "s_mov_b32 s63, 0\n\t"
    "s_mov_b64 vcc, 0\n\t"
    "LQG_%=:\n\t"
    "v_smfmac_f32_16x16x64_bf16 v[48:51], v[56:59], v[32:39], v5\n\t"
    "v_smfmac_f32_16x16x64_bf16 v[52:55], v[56:59], v[40:47], v5\n\t"
    "s_bitcmp1_b32 s60, 4\n\t"
    "s_nop 6\n\t"
    "v_permlane32_swap_b32 v48, v52\n\t"
    "v_permlane32_swap_b32 v49, v53\n\t"
    "v_permlane32_swap_b32 v50, v54\n\t"
    "v_permlane32_swap_b32 v51, v55\n\t"
    "v_add_f32_dpp v26, v52, v48 row_ror:8 row_mask:0xf bank_mask:0x3\n\t"
    "v_add_f32_dpp v27, v53, v49 row_ror:8 row_mask:0xf bank_mask:0x3\n\t"
    "v_add_f32_dpp v28, v54, v50 row_ror:8 row_mask:0xf bank_mask:0x3\n\t"
    "v_add_f32_dpp v29, v55, v51 row_ror:8 row_mask:0xf bank_mask:0x3\n\t"
    "v_cvt_pk_bf16_f32 v56, v26, v27\n\t"
    "v_cvt_pk_bf16_f32 v57, v28, v29\n\t"
    "s_cbranch_scc1 LQOUT_%=\n\t"
    "ds_write_b64 v21, v[56:57]\n\t"
    "ds_read_b128 v[32:35], v22 offset:0\n\t"
    "ds_read_b128 v[36:39], v22 offset:16\n\t"
    "ds_read_b128 v[40:43], v22 offset:32\n\t"
    "ds_read_b128 v[44:47], v22 offset:48\n\t"
    "v_add_f32 v30, v26, v27\n\t"
    "v_mul_f32 v31, v26, v26\n\t"
    "v_add_f32 v52, v28, v29\n\t"
    "v_fmac_f32 v31, v27, v27\n\t"
    "v_add_f32 v30, v30, v52\n\t"
    "v_fmac_f32 v31, v28, v28\n\t"
    "v_fmac_f32 v31, v29, v29\n\t"
    "v_mov_b32 v48, 0\n\t"
    "v_mov_b32 v49, 0\n\t"
    "v_mov_b32_dpp v30, v31 row_ror:8 row_mask:0xf bank_mask:0xc\n\t"
    "v_mov_b32 v50, 0\n\t"
    "v_mov_b32 v51, 0\n\t"
    "v_add_f32_dpp v30, v30, v30 quad_perm:[1,0,3,2] row_mask:0xf bank_mask:0xf\n\t"
    "s_waitcnt lgkmcnt(3)\n\t"
    "v_and_or_b32 v32, v32, v61, v62\n\t"
    "v_and_or_b32 v33, v33, v61, v62\n\t"
    "v_and_or_b32 v34, v34, v61, v62\n\t"
    "v_and_or_b32 v35, v35, v61, v62\n\t"
    "s_waitcnt lgkmcnt(2)\n\t"
    "v_and_or_b32 v36, v36, v61, v62\n\t"
    "v_smfmac_f32_16x16x64_bf16 v[48:51], v[32:35], v[64:71], v5\n\t"
    "v_and_or_b32 v37, v37, v61, v62\n\t"
    "v_and_or_b32 v38, v38, v61, v62\n\t"
    "v_and_or_b32 v39, v39, v61, v62\n\t"
    "s_waitcnt lgkmcnt(1)\n\t"
    "v_and_or_b32 v40, v40, v61, v62\n\t"
    "v_smfmac_f32_16x16x64_bf16 v[48:51], v[36:39], v[72:79], v5\n\t"
    "v_and_or_b32 v41, v41, v61, v62\n\t"
    "v_and_or_b32 v42, v42, v61, v62\n\t"
    "v_and_or_b32 v43, v43, v61, v62\n\t"
    "s_waitcnt lgkmcnt(0)\n\t"
    "v_and_or_b32 v44, v44, v61, v62\n\t"
    "v_smfmac_f32_16x16x64_bf16 v[48:51], v[40:43], v[80:87], v5\n\t"
    "v_and_or_b32 v45, v45, v61, v62\n\t"
    "v_and_or_b32 v46, v46, v61, v62\n\t"
    "v_and_or_b32 v47, v47, v61, v62\n\t"
    "v_add_f32_dpp v30, v30, v30 quad_perm:[2,3,0,1] row_mask:0xf bank_mask:0xf\n\t"
    "s_cmp_eq_u32 s84, 0\n\t"
    "v_smfmac_f32_16x16x64_bf16 v[48:51], v[44:47], v[88:95], v5\n\t"
    "v_add_f32_dpp v30, v30, v30 row_half_mirror row_mask:0xf bank_mask:0xf\n\t"
    "v_mov_b32 v31, v30\n\t"
    "ds_write_b128 v17, v[26:29] offset:2048\n\t"
    "s_nop 0\n\t"
    "v_permlane16_swap_b32 v31, v30\n\t"
    "v_add_f32 v30, v30, v31\n\t"
    "v_mov_b32 v31, v30\n\t"
    "s_nop 0\n\t"
    "v_permlane16_swap_b32 v48, v50\n\t"
    "v_permlane16_swap_b32 v49, v51\n\t"
    "v_permlane32_swap_b32 v31, v30\n\t"
    "v_add_f32_dpp v52, v50, v48 row_ror:8 row_mask:0xf bank_mask:0xf\n\t"
    "v_add_f32_dpp v53, v51, v49 row_ror:8 row_mask:0xf bank_mask:0xf\n\t"
    "v_add_f32 v30, v30, v31\n\t"
    "ds_write_b64 v23, v[52:53]\n\t"
    "ds_write_b32 v20, v30\n\t"
    "v_mov_b32 v30, 160320\n\t"
    "s_waitcnt lgkmcnt(0)\n\t"
    "s_cbranch_vccnz LCR_%=\n\t"
    "s_cbranch_execz LNB16_%=\n\t"
    "s_barrier\n\t"
    "LNB16_%=:\n\t"
    "s_cbranch_scc0 LCR_%=\n\t"
    "LCQF_%=:\n\t"
    "ds_read_b128 v[48:51], v30\n\t"
    "ds_read_b128 v[52:55], v30 offset:16\n\t"
    "ds_read_b128 v[32:35], v24 offset:0\n\t"
    "ds_read_b128 v[36:39], v24 offset:256\n\t"
    "ds_read_b128 v[40:43], v24 offset:512\n\t"
    "ds_read_b128 v[44:47], v24 offset:768\n\t"
    "s_mov_b32 s74, 0x3b124925\n\t"
    "s_waitcnt lgkmcnt(4)\n\t"
    "v_pk_add_f32 v[48:49], v[48:49], v[50:51]\n\t"
    "v_pk_add_f32 v[52:53], v[52:53], v[54:55]\n\t"
    "v_pk_add_f32 v[48:49], v[48:49], v[52:53]\n\t"
    "v_mul_f32 v48, s74, v48\n\t"
    "v_mul_f32 v50, v48, v48\n\t"
    "v_fma_f32 v49, v49, s74, -v50\n\t"
    "v_max_f32 v49, 0, v49\n\t"
    "v_add_f32 v49, 0x3727c5ac, v49\n\t"
    "v_rsq_f32 v49, v49\n\t"
    "s_waitcnt lgkmcnt(2)\n\t"
    "v_pk_add_f32 v[32:33], v[32:33], v[36:37]\n\t"
    "v_pk_add_f32 v[34:35], v[34:35], v[38:39]\n\t"
    "v_mul_f32 v49, 0x3db8aa3b, v49\n\t"
    "s_waitcnt lgkmcnt(0)\n\t"
    "v_pk_add_f32 v[40:41], v[40:41], v[44:45]\n\t"
    "v_pk_add_f32 v[42:43], v[42:43], v[46:47]\n\t"
    "v_pk_add_f32 v[32:33], v[32:33], v[40:41]\n\t"
    "v_pk_add_f32 v[34:35], v[34:35], v[42:43]\n\t"
    "v_fma_f32 v32, -v48, v34, v32\n\t"
    "v_fma_f32 v33, -v48, v35, v33\n\t"
    "v_mul_f32 v32, v32, v49\n\t"
    "v_mul_f32 v33, v33, v49\n\t"
    "v_cvt_pk_bf16_f32 v32, v32, v33\n\t"
    "v_min_u32 v32, -2, v32\n\t"
    "s_cbranch_execz LCR_%=\n\t"
    "buffer_store_dword v32, v25, s[24:27], s73 offen sc1\n\t"
    "s_branch LCR_%=\n\t"
    "LQZ_%=:\n\t"
    "v_mov_b32 v32, 0\n\t"
    "v_mov_b32 v33, 0\n\t"
    "v_mov_b32 v34, 0\n\t"
    "v_mov_b32 v35, 0\n\t"
    "v_mov_b32 v36, 0\n\t"
    "v_mov_b32 v37, 0\n\t"
    "v_mov_b32 v38, 0\n\t"
    "v_mov_b32 v39, 0\n\t"
    "v_mov_b32 v40, 0\n\t"
    "v_mov_b32 v41, 0\n\t"
    "v_mov_b32 v42, 0\n\t"
    "v_mov_b32 v43, 0\n\t"
    "v_mov_b32 v44, 0\n\t"
    "v_mov_b32 v45, 0\n\t"
    "v_mov_b32 v46, 0\n\t"
    "v_mov_b32 v47, 0\n\t"
    "s_nop 0\n\t"
    "s_branch LQG_%=\n\t"
    "LQOUT_%=:\n\t"
    "s_cbranch_execz LCR_%=\n\t"
    "buffer_store_dwordx2 v[56:57], v60, s[44:47], 0 offen\n\t"
    "s_branch LCR_%=\n\t"
    "LPQ_%=:\n\t"
    "s_add_u32 s72, s57, s58\n\t"
    "s_cbranch_execz LPQC_%=\n\t"
    "LPQ1_%=:\n\t"
    "buffer_load_dwordx4 v[48:51], v13, s[24:27], s72 offen sc1\n\t"
    "s_waitcnt vmcnt(0)\n\t"
    "LPQC_%=:\n\t"
    "v_max3_u32 v30, v48, v49, v50\n\t"
    "v_max_u32 v30, v51, v30\n\t"
    "v_cmp_eq_u32 vcc, -1, v30\n\t"
    "s_cbranch_vccz LPQT_%=\n\t"
    "s_sub_u32 s63, s63, 1\n\t"
    "s_cbranch_scc0 LPQ1_%=\n\t"
    "s_mov_b32 s63, 0\n\t"
    "s_mov_b64 vcc, 0\n\t"
    "LPQT_%=:\n\t"
    "ds_write_b128 v14, v[48:51]\n\t"
    "ds_read_b128 v[32:35], v15 offset:0\n\t"
    "ds_read_b128 v[36:39], v15 offset:128\n\t"
    "ds_read_b128 v[40:43], v15 offset:256\n\t"
    "ds_read_b128 v[44:47], v15 offset:384\n\t"
    "s_setpc_b64 s[66:67]\n\t"
    "LEND_%=:\n\t"
    :
    : "{s[8:11]}"(rKA), "{s[12:15]}"(rVA), "{s[16:19]}"(rKB), "{s[20:23]}"(rVB), "{s[24:27]}"(rWS), "{s[28:31]}"(rW0), "{s[32:35]}"(rW1), "{s[44:47]}"(rOUT),
      "{s39}"(vote), "{s[40:43]}"(mk(is_q ? wqB : wqA, W_BYTES)), "{s89}"(wbq), "{s90}"(rot), "{s91}"(pre), "{s48}"(sa), "{s49}"(sb), "{s50}"(w), "{s51}"(role), "{s52}"(wb), "{s53}"(c), "{s88}"(t00),
      "{v[0:3]}"(v4u{vK, vV, vLA, vM0}), "{v[4:7]}"(v4u{vM1, vIDX, 0u, vMW}), "{v[8:11]}"(v4u{vMR, vPOst, vMLst, vMLX}), "{v[12:15]}"(v4u{vMLXw, vQg, vQw, vQr}),
      "{v16}"(vW), "{v17}"(vTAB), "{v62}"(vWq), "{v63}"(vPRO)
    : "memory", "vcc", "scc", "s38", "s54", "s55", "s56", "s57", "s58", "s59", "s60", "s61", "s62", "s63", "s64", "s65", "s66", "s67", "s68", "s69", "s70", "s71", "s72", "s73", "s74", "s75", "s76", "s77", "s78", "s79", "s80", "s81", "s82", "s83", "s84", "s85", "s86", "s87", "s92", "s93", "s94", "s95", "s96", "s97", "s98", "s99", "v18", "v19", "v20", "v21", "v22", "v23", "v24", "v25", "v26", "v27", "v28", "v29", "v30", "v31", "v32", "v33", "v34", "v35", "v36", "v37", "v38", "v39", "v40", "v41", "v42", "v43", "v44", "v45", "v46", "v47", "v48", "v49", "v50", "v51", "v52", "v53", "v54", "v55", "v56", "v57", "v58", "v59", "v60", "v61");
}
}

extern "C" void kernel_launch(void* const* d_in, const int* in_sizes, int n_in, void* d_out, int out_size, void* d_ws, size_t ws_size, hipStream_t stream) {
  (void)in_sizes; (void)n_in; (void)out_size; (void)ws_size;
  (void)hipFuncSetAttribute((const void*)fk::fused, hipFuncAttributeMaxDynamicSharedMemorySize, fk::LDS_BYTES);
  fk::fused<<<256, 256, fk::LDS_BYTES, stream>>>((const char*)d_in[0], (const char*)d_in[1], (const char*)d_in[2], (const char*)d_in[3], (const char*)d_in[4],
                                                  (const char*)d_in[6], (const char*)d_in[7], (const char*)d_in[8], (const char*)d_in[9], (char*)d_out, (char*)d_ws);
}
```
